```python
import torch

def _softmax(x, axis=-1):
    return torch.softmax(x, dim=axis)
def _gelu(x):
    return torch.nn.functional.gelu(x, approximate="tanh")
def _silu(x):
    return torch.nn.functional.silu(x)
def _sigmoid(x):
    return torch.sigmoid(x)
def _layernorm(x, axis=-1, eps=1e-5):
    m = x.mean(dim=axis, keepdim=True)
    v = x.var(dim=axis, keepdim=True, unbiased=False)
    return (x - m) / torch.sqrt(v + eps)
def _scatter_add(tgt, idx, src, axis=0):
    return tgt.clone().index_add_(axis, idx, src)
def _segment_sum(src, seg, n):
    out = torch.zeros((n,) + src.shape[1:], dtype=src.dtype)
    return out.index_add_(0, seg, src)
def _pad(x, pw):
    flat = tuple(v for pair in reversed(pw) for v in pair)
    return torch.nn.functional.pad(x, flat)

kernel_name = 'recurrent_scan__ssm_conv_s4__mixer_s_mlp__prompt'

def setup_inputs():

    _w = (lambda a: (lambda t: t.to(torch.bfloat16) if t.is_floating_point() else t)(torch.as_tensor(a)))
    def _A_setup():
        B, L, D, N = 1, 2048, 448, 256
        A_re = -(lambda _x: torch.exp(torch.as_tensor(_x)))((lambda *s: torch.randn(*s, dtype=torch.bfloat16) * 3.2)(D, N))
        A_im = torch.pi * (lambda *s: torch.randn(*s, dtype=torch.bfloat16) * 0.05)(D, N)
        B_re = (lambda *s: torch.randn(*s, dtype=torch.bfloat16) * 0.1)(D, N)
        B_im = (lambda *s: torch.randn(*s, dtype=torch.bfloat16) * 0.1)(D, N)
        C_re = (lambda *s: torch.randn(*s, dtype=torch.bfloat16) * 0.1)(D, N)
        C_im = (lambda *s: torch.randn(*s, dtype=torch.bfloat16) * 0.1)(D, N)
        log_dt = (lambda *s: torch.randn(*s, dtype=torch.bfloat16) * 0.05)(D) * 0.5 - 4.0
        Dskip = (lambda *s: torch.randn(*s, dtype=torch.bfloat16) * 0.05)(D)
        return {
            "u": _w((lambda *s: torch.randn(*s, dtype=torch.bfloat16) * 0.05)(B, L, D)),
            "A_re": _w(A_re),
            "A_im": _w(A_im),
            "B_re": _w(B_re),
            "B_im": _w(B_im),
            "C_re": _w(C_re),
            "C_im": _w(C_im),
            "log_dt": _w(log_dt),
            "Dskip": _w(Dskip),
        }
    _A_in = _A_setup()

    def _B_setup():
        B, S, D, F = 1, 2048, 448, 1792
        return {
            "x":  _w((lambda *s: torch.randn(*s, dtype=torch.bfloat16) * 0.05)(B, S, D)),
            "w1": _w((lambda *s: torch.randn(*s, dtype=torch.bfloat16) * 3.2)(D, D) * (D ** -0.5)),
            "w2": _w((lambda *s: torch.randn(*s, dtype=torch.bfloat16) * 3.2)(D, D) * (D ** -0.5)),
            "w3": _w((lambda *s: torch.randn(*s, dtype=torch.bfloat16) * 3.2)(D, F) * (D ** -0.5)),
            "w4": _w((lambda *s: torch.randn(*s, dtype=torch.bfloat16) * 3.2)(F, D) * (F ** -0.5)),
        }
    _B_in = _B_setup()

    return {
        "A_u": _A_in["u"],
        "A_A_re": _A_in["A_re"],
        "A_A_im": _A_in["A_im"],
        "A_B_re": _A_in["B_re"],
        "A_B_im": _A_in["B_im"],
        "A_C_re": _A_in["C_re"],
        "A_C_im": _A_in["C_im"],
        "A_log_dt": _A_in["log_dt"],
        "A_Dskip": _A_in["Dskip"],
        "B_x": _B_in["x"],
        "B_w1": _B_in["w1"],
        "B_w2": _B_in["w2"],
        "B_w3": _B_in["w3"],
        "B_w4": _B_in["w4"],
    }

def reference(A_u, A_A_re, A_A_im, A_B_re, A_B_im, A_C_re, A_C_im, A_log_dt, A_Dskip, B_x, B_w1, B_w2, B_w3, B_w4):
    def _A_ref(u, A_re, A_im, B_re, B_im, C_re, C_im, log_dt, Dskip):
        Bsz, L, D = u.shape
        dt = torch.exp(log_dt)[:, None]
        A = A_re + 1j * A_im
        Bc = B_re + 1j * B_im
        Cc = C_re + 1j * C_im
        logdA = dt * A
        dB = dt * Bc
        l = (lambda x, dt: x.to(dt))(torch.arange(L), A_re.dtype)
        P = torch.exp(logdA[:, :, None] * l[None, None, :])
        K = torch.einsum("dn,dnl,dn->dl", Cc, P, dB).real
        L2 = 2 * L
        Kp = _pad(K, ((0, 0), (0, L)))
        up = (lambda x, *d: x.permute(*d))(u, 0, 2, 1)
        up = _pad(up, ((0, 0), (0, 0), (0, L)))
        Kf = (lambda x, axes=(-2,-1): torch.fft.rfft2(x.float(), dim=axes))(Kp[:, None, :], axes=(-2, -1))
        Uf = (lambda x, axes=(-2,-1): torch.fft.rfft2(x.float(), dim=axes))(up[:, :, None, :], axes=(-2, -1))
        Yf = Uf * Kf[None, :, :, :]
        y = (lambda x, s, axes=(-2,-1): torch.fft.irfft2(x, s=s, dim=axes).to(torch.bfloat16))(Yf, (1, L2), axes=(-2, -1))[:, :, 0, :L]
        y = (lambda x, *d: x.permute(*d))(y, 0, 2, 1) + u * Dskip[None, None, :]
        return y

    def _B_ref(x, w1, w2, w3, w4):
        B, S, D = x.shape
        h = _gelu(_layernorm(x, axis=-1) @ w1)
        c = D // 4
        g0 = _pad(h[:, :, 0*c:1*c], ((0, 0), (1, 0), (0, 0)))[:, :S, :]
        g1 = _pad(h[:, :, 1*c:2*c], ((0, 0), (0, 1), (0, 0)))[:, 1:, :]
        g2 = _pad(h[:, :, 2*c:3*c], ((0, 0), (2, 0), (0, 0)))[:, :S, :]
        g3 = _pad(h[:, :, 3*c:   ], ((0, 0), (0, 2), (0, 0)))[:, 2:, :]
        hs = torch.concatenate([g0, g1, g2, g3], axis=-1) @ w2
        x1 = x + hs
        h2 = _gelu(_layernorm(x1, axis=-1) @ w3) @ w4
        return x1 + h2

    x = A_u
    xn = _layernorm(x, axis=-1)
    s = _A_ref(xn, A_A_re, A_A_im, A_B_re, A_B_im, A_C_re, A_C_im, A_log_dt, A_Dskip)
    g = _silu(_B_ref(xn, B_w1, B_w2, B_w3, B_w4))
    return x + s * g
```

```cpp
#include <hip/hip_runtime.h>
#include <hip/hip_bf16.h>
#include <stdint.h>
typedef unsigned short u16;
typedef short bf16x8 __attribute__((ext_vector_type(8)));
typedef float f32x4 __attribute__((ext_vector_type(4)));
constexpr int L = 2048, D = 448, NS = 256, F = 1792;
__device__ __forceinline__ float bf2f(u16 b) { return __uint_as_float((unsigned)b << 16); }
__device__ __forceinline__ u16 f2bf(float f) { unsigned u = __float_as_uint(f); u += 0x7fffu + ((u >> 16) & 1u); return (u16)(u >> 16); }
__device__ __forceinline__ float rbf(float f) { return bf2f(f2bf(f)); }
__device__ __forceinline__ float gelu_t(float x) { float u = 0.7978845608028654f * (x + 0.044715f * x * x * x); return x / (1.f + __expf(-2.f * u)); }
__device__ __forceinline__ float wsum(float v) { for (int o = 32; o; o >>= 1) v += __shfl_xor(v, o); return v; }

__device__ __forceinline__ void ln7(float (&v)[7]) {
  float s = 0; for (int i = 0; i < 7; ++i) s += v[i];
  float m = wsum(s) * (1.f / D);
  float q = 0; for (int i = 0; i < 7; ++i) q += (v[i] - m) * (v[i] - m);
  float var = rbf(wsum(q) * (1.f / D));
  float mb = rbf(m);
  float sd = rbf(sqrtf(rbf(var + 1e-5f)));
  for (int i = 0; i < 7; ++i) v[i] = rbf(rbf(v[i] - mb) / sd);
}
__global__ void k_ln(const u16* x, u16* o1, u16* o2, int two) {
  int row = blockIdx.x * 4 + (threadIdx.x >> 6), lane = threadIdx.x & 63;
  float v[7]; for (int i = 0; i < 7; ++i) v[i] = bf2f(x[row * D + lane + 64 * i]);
  ln7(v);
  for (int i = 0; i < 7; ++i) o1[row * D + lane + 64 * i] = f2bf(v[i]);
  if (two) { ln7(v); for (int i = 0; i < 7; ++i) o2[row * D + lane + 64 * i] = f2bf(v[i]); }
}
template <int EPI>
__global__ void __launch_bounds__(256) k_gemm(const u16* A, const u16* W, u16* out, int K, int N,
                                              const u16* r1, const u16* xin, const float* y, const u16* xn, const u16* dskip) {
  __shared__ u16 wt[64][40];
  int tid = threadIdx.x, lane = tid & 63, w = tid >> 6;
  int m0 = blockIdx.y * 64, n0 = blockIdx.x * 64;
  int wm = (w >> 1) * 32, wn = (w & 1) * 32;
  f32x4 acc[2][2] = {};
  for (int k0 = 0; k0 < K; k0 += 32) {
    __syncthreads();
    { int k = tid >> 3, c = (tid & 7) * 8;
      uint4 v = *(const uint4*)(W + (size_t)(k0 + k) * N + n0 + c);
      const u16* p = (const u16*)&v;
      for (int j = 0; j < 8; ++j) wt[c + j][k] = p[j]; }
    __syncthreads();
    bf16x8 a[2], b[2];
    for (int i = 0; i < 2; ++i) {
      int r = m0 + wm + i * 16 + (lane & 15), kk = k0 + (lane >> 4) * 8;
      if (EPI == 1) {
        int q = kk / 112; int sh = q == 0 ? -1 : q == 1 ? 1 : q == 2 ? -2 : 2;
        int rr = r + sh;
        if (rr < 0 || rr >= L) a[i] = bf16x8{0,0,0,0,0,0,0,0}; else a[i] = *(const bf16x8*)(A + (size_t)rr * K + kk);
      } else a[i] = *(const bf16x8*)(A + (size_t)r * K + kk);
      b[i] = *(const bf16x8*)(&wt[wn + i * 16 + (lane & 15)][(lane >> 4) * 8]);
    }
    for (int i = 0; i < 2; ++i) for (int j = 0; j < 2; ++j)
      acc[i][j] = __builtin_amdgcn_mfma_f32_16x16x32_bf16(a[i], b[j], acc[i][j], 0, 0, 0);
  }
  for (int i = 0; i < 2; ++i) for (int j = 0; j < 2; ++j) for (int e = 0; e < 4; ++e) {
    int r = m0 + wm + i * 16 + (lane >> 4) * 4 + e, c = n0 + wn + j * 16 + (lane & 15);
    float v = rbf(acc[i][j][e]);
    size_t o = (size_t)r * N + c;
    if (EPI == 0) out[o] = f2bf(gelu_t(v));
    else if (EPI == 1) out[o] = f2bf(bf2f(r1[o]) + v);
    else {
      float z = rbf(bf2f(r1[o]) + v);
      float g = rbf(z / (1.f + __expf(-z)));
      float s = rbf(rbf(y[(size_t)c * L + r]) + rbf(bf2f(xn[o]) * bf2f(dskip[c])));
      out[o] = f2bf(bf2f(xin[o]) + rbf(s * g));
    }
  }
}
__global__ void __launch_bounds__(256) k_kgen(const u16* Are, const u16* Aim, const u16* Bre, const u16* Bim, const u16* Cre, const u16* Cim,
                                              const u16* logdt, float* Kout) {
  __shared__ float pa[NS], pb[NS], wr[NS], wi[NS];
  __shared__ float kc[648];
  int d = blockIdx.x, t = threadIdx.x;
  float dt = rbf(expf(bf2f(logdt[d])));
  { int o = d * NS + t;
    pa[t] = dt * bf2f(Are[o]); pb[t] = dt * bf2f(Aim[o]);
    float br = dt * bf2f(Bre[o]), bi = dt * bf2f(Bim[o]), cr = bf2f(Cre[o]), ci = bf2f(Cim[o]);
    wr[t] = cr * br - ci * bi; wi[t] = cr * bi + ci * br; }
  __syncthreads();
  for (int i = t; i < 641; i += 256) {
    int l = i <= 256 ? i : i <= 384 ? 256 + 2 * (i - 256) : i <= 512 ? 512 + 4 * (i - 384) : 1024 + 8 * (i - 512);
    float fl = (float)l, s = 0;
    for (int n = 0; n < NS; ++n) {
      float e = __expf(pa[n] * fl);
      float rev = pb[n] * fl * 0.15915494309189535f; rev -= floorf(rev);
      float sn = __builtin_amdgcn_sinf(rev), cs = __builtin_amdgcn_cosf(rev);
      s += e * (wr[n] * cs - wi[n] * sn);
    }
    kc[i] = s;
  }
  __syncthreads();
  for (int l = t; l < L; l += 256) {
    int v = (int)rbf((float)l);
    int i = v <= 256 ? v : v <= 512 ? 256 + (v - 256) / 2 : v <= 1024 ? 384 + (v - 512) / 4 : 512 + (v - 1024) / 8;
    Kout[d * L + l] = kc[i];
  }
}
__global__ void __launch_bounds__(256) k_conv(const float* Kin, const u16* xn, float* y) {
  __shared__ float ks[L], us[L + 8];
  int d = blockIdx.x, t = threadIdx.x;
  for (int l = t; l < L; l += 256) { ks[l] = Kin[d * L + l]; us[l + 8] = bf2f(xn[(size_t)l * D + d]); }
  if (t < 8) us[t] = 0;
  __syncthreads();
  for (int h = 0; h < 2; ++h) {
    int tt = h == 0 ? t : 511 - t;
    float a0 = 0, a1 = 0, a2 = 0, a3 = 0;
    int t0 = 4 * tt;
    float u1 = us[8 + t0 + 1], u2 = us[8 + t0 + 2], u3 = us[8 + t0 + 3];
    for (int j = 0; j <= t0 + 3; ++j) {
      float k = ks[j]; int idx = t0 - j; float u0 = idx >= -8 ? us[8 + idx] : 0.f;
      a0 += k * u0; a1 += k * u1; a2 += k * u2; a3 += k * u3;
      u3 = u2; u2 = u1; u1 = u0;
    }
    y[(size_t)d * L + t0] = a0; y[(size_t)d * L + t0 + 1] = a1; y[(size_t)d * L + t0 + 2] = a2; y[(size_t)d * L + t0 + 3] = a3;
  }
}
__global__ void k_empty(int* p) { if (p == nullptr) __builtin_trap(); }
#define NEMPTY 0
extern "C" void kernel_launch(void* const* d_in, const int* in_sizes, int n_in, void* d_out, int out_size, void* d_ws, size_t ws_size, hipStream_t stream) {
  const u16* x = (const u16*)d_in[0];
  const u16 *Are = (const u16*)d_in[1], *Aim = (const u16*)d_in[2], *Bre = (const u16*)d_in[3], *Bim = (const u16*)d_in[4], *Cre = (const u16*)d_in[5], *Cim = (const u16*)d_in[6];
  const u16 *logdt = (const u16*)d_in[7], *dskip = (const u16*)d_in[8];
  const u16 *w1 = (const u16*)d_in[10], *w2 = (const u16*)d_in[11], *w3 = (const u16*)d_in[12], *w4 = (const u16*)d_in[13];
  char* ws = (char*)d_ws;
  const size_t T = (size_t)L * D * 2;
  u16* xn = (u16*)ws; u16* xnn = (u16*)(ws + T); u16* h = (u16*)(ws + 2 * T); u16* x1 = (u16*)(ws + 3 * T); u16* x1n = (u16*)(ws + 4 * T);
  u16* H = (u16*)(ws + 5 * T); float* Kf = (float*)(ws + 9 * T); float* y = (float*)(ws + 11 * T);
  k_ln<<<L / 4, 256, 0, stream>>>(x, xn, xnn, 1);
  k_kgen<<<D, 256, 0, stream>>>(Are, Aim, Bre, Bim, Cre, Cim, logdt, Kf);
  k_conv<<<D, 256, 0, stream>>>(Kf, xn, y);
  k_gemm<0><<<dim3(D / 64, L / 64), 256, 0, stream>>>(xnn, w1, h, D, D, nullptr, nullptr, nullptr, nullptr, nullptr);
  k_gemm<1><<<dim3(D / 64, L / 64), 256, 0, stream>>>(h, w2, x1, D, D, xn, nullptr, nullptr, nullptr, nullptr);
  k_ln<<<L / 4, 256, 0, stream>>>(x1, x1n, nullptr, 0);
  k_gemm<0><<<dim3(F / 64, L / 64), 256, 0, stream>>>(x1n, w3, H, D, F, nullptr, nullptr, nullptr, nullptr, nullptr);
  for (int i = 0; i < NEMPTY; ++i) k_empty<<<256, 256, 0, stream>>>((int*)ws);
  k_gemm<2><<<dim3(D / 64, L / 64), 256, 0, stream>>>(H, w4, (u16*)d_out, F, D, x1, x, y, xn, dskip);
}
```

```cpp
#include <hip/hip_runtime.h>
#include <stdint.h>

typedef unsigned short u16;
typedef unsigned u32;
typedef unsigned long long u64;
typedef float f32x2 __attribute__((ext_vector_type(2)));
typedef float f32x4 __attribute__((ext_vector_type(4)));
typedef u32 u32x2 __attribute__((ext_vector_type(2)));
typedef u32 u32x4 __attribute__((ext_vector_type(4)));
typedef short s16x4 __attribute__((ext_vector_type(4)));
typedef __bf16 bf16x2 __attribute__((ext_vector_type(2)));
typedef __bf16 bf16x8 __attribute__((ext_vector_type(8)));
typedef _Float16 f16x2 __attribute__((ext_vector_type(2)));
typedef _Float16 f16x8 __attribute__((ext_vector_type(8)));
#define GAS __attribute__((address_space(1)))
#define LAS __attribute__((address_space(3)))
#define DI __device__ __forceinline__
typedef GAS u64 gu64;

constexpr int L = 2048, D = 448, NS = 256, F = 1792;
constexpr int NWG = 256;
constexpr u32 WS_E = 0;
constexpr u32 WS_FH = 4096, WS_FX = WS_FH + 4096, WS_FA = WS_FX + 4096, WS_FB = WS_FA + 4096;
constexpr u32 WS_FU = WS_FB + 4096, WS_FS = WS_FU + 2048;
constexpr u32 WS_STAMP = 64u << 20;
constexpr u32 HROW = 2064 * 16;
constexpr u32 WS_H = 65536;
constexpr u32 WS_X1 = WS_H + 56 * HROW;
constexpr u32 WS_U = WS_X1 + 56 * HROW;
constexpr u32 WS_S = WS_U;
constexpr u32 WS_ST = WS_S + 448 * 4096;
constexpr u32 WS_H1 = WS_ST + 224 * 1024, WS_H3 = WS_H1 + 56 * HROW;
constexpr u32 WS_END = WS_H3 + 56 * HROW;
constexpr u32 LC_REC = 0;
constexpr u32 LC_STA = 9216;
constexpr u32 LC_STB = LC_STA + 1024;
constexpr u32 LC_E = LC_STB + 512;
constexpr u32 LC_PF = LC_E + 256;
constexpr u32 LC_PL = LC_PF + 256;
constexpr u32 LC_STX = LC_PL + 256;
constexpr u32 LC_SW = LC_STX + 2048;
constexpr u32 LC_SCR = 14336;
constexpr u32 LA = 49152, LB = LA + 57344, LDS_BYTES = 163840;
constexpr u32 LT_S = LC_REC + 512, LR = 16384;
static_assert(LC_SW + 32 <= LC_SCR && LC_SCR + 34816 == LA && LB + 57344 == LDS_BYTES, "LDS map");

DI float bf_lo(u32 v) { return __uint_as_float(v << 16); }
DI float bf_hi(u32 v) { return __uint_as_float(v & 0xffff0000u); }
DI f32x2 bf2(u32 v) { return (f32x2){bf_lo(v), bf_hi(v)}; }
DI u32 pk_bf(f32x2 a) { return __builtin_bit_cast(u32, __builtin_convertvector(a, bf16x2)); }
DI u32 pk_h(f32x2 a) { return __builtin_bit_cast(u32, __builtin_convertvector(a, f16x2)); }
DI f32x2 h2f(u32 v) { return __builtin_convertvector(__builtin_bit_cast(f16x2, v), f32x2); }
DI u32 hadd(u32 a, u32 b) { return __builtin_bit_cast(u32, __builtin_bit_cast(f16x2, a) + __builtin_bit_cast(f16x2, b)); }
DI float rbf(float a) { return bf_lo(pk_bf((f32x2){a, a})); }
DI f32x2 lo2(f32x4 v) { return (f32x2){v.x, v.y}; }
DI f32x2 hi2(f32x4 v) { return (f32x2){v.z, v.w}; }
DI f32x2 bc(float a) { return (f32x2){a, a}; }
DI f32x2 fma2(f32x2 a, f32x2 b, f32x2 c) { return __builtin_elementwise_fma(a, b, c); }
DI float dot2(u32 a, u32 b, float c) { return __builtin_amdgcn_fdot2_f32_bf16(__builtin_bit_cast(bf16x2, a), __builtin_bit_cast(bf16x2, b), c, false); }
DI f32x4 mfma_bf(u32x4 a, u32x4 b, f32x4 c) { return __builtin_amdgcn_mfma_f32_16x16x32_bf16(__builtin_bit_cast(bf16x8, a), __builtin_bit_cast(bf16x8, b), c, 0, 0, 0); }
DI f32x4 mfma_h(u32x4 a, u32x4 b, f32x4 c) { return __builtin_amdgcn_mfma_f32_16x16x32_f16(__builtin_bit_cast(f16x8, a), __builtin_bit_cast(f16x8, b), c, 0, 0, 0); }
constexpr float GC1 = -2.3022082f, GC2 = -0.10294324f;
DI f32x2 gelu2(f32x2 acc, f32x2 cs, f32x4 c) {
  const f32x2 a = fma2(cs, bc(-c.x), acc);
  f32x2 q;
  asm("v_pk_fma_f32 %0, %1, %2, %3 op_sel:[1,0,1]" : "=v"(q) : "v"(hi2(c)), "v"(a * a), "v"(lo2(c)));
  const f32x2 t = a * q;
  const f32x2 d = fma2((f32x2){__builtin_amdgcn_exp2f(t.x), __builtin_amdgcn_exp2f(t.y)}, bc(c.z), bc(c.z));
  return a * (f32x2){__builtin_amdgcn_rcpf(d.x), __builtin_amdgcn_rcpf(d.y)};
}
DI float fma1(float a, float b, float c) { float d; asm("v_fma_f32 %0, %1, %2, %3" : "=v"(d) : "v"(a), "v"(b), "v"(c)); return d; }
DI float fnma1(float a, float b, float c) { float d; asm("v_fma_f32 %0, -%1, %2, %3" : "=v"(d) : "v"(a), "v"(b), "v"(c)); return d; }
DI float mul1(float a, float b) { float d; asm("v_mul_f32 %0, %1, %2" : "=v"(d) : "v"(a), "v"(b)); return d; }
DI float add1(float a, float b) { float d; asm("v_add_f32 %0, %1, %2" : "=v"(d) : "v"(a), "v"(b)); return d; }
DI float fma1t(float a, float b, float c) { float d; asm("s_nop 0\n\tv_fma_f32 %0, %1, %2, %3" : "=v"(d) : "v"(a), "v"(b), "v"(c)); return d; }
DI float mul1t(float a, float b) { float d; asm("s_nop 0\n\tv_mul_f32 %0, %1, %2" : "=v"(d) : "v"(a), "v"(b)); return d; }
DI float add1t(float a, float b) { float d; asm("s_nop 0\n\tv_add_f32 %0, %1, %2" : "=v"(d) : "v"(a), "v"(b)); return d; }
template <typename T> DI T lds_ld(u32 a) { return *(const LAS T*)(size_t)a; }
template <typename T> DI void lds_st(u32 a, T v) { *(LAS T*)(size_t)a = v; }
DI u32x2 lds_tr(u32 a) { return __builtin_bit_cast(u32x2, __builtin_amdgcn_ds_read_tr16_b64_v4i16((LAS s16x4*)(size_t)a)); }
DI u32x4 lds_tr2(u32 a, u32 b) { const u32x2 x = lds_tr(a), y = lds_tr(b); return (u32x4){x.x, x.y, y.x, y.y}; }
DI u32 opq(u32 a) { asm("" : "+v"(a)); return a; }
DI u32x4 mk_srd(const void* p, u32 bytes) { u32x4 r = {(u32)(size_t)p, (u32)((size_t)p >> 32), bytes, 0x00020000u}; asm("" : "+s"(r)); return r; }
#define DMA_MUL(pol, so, r, voff, lb, li, c, k) asm volatile("s_add_u32 m0, %1, %c2\n\ts_mul_i32 %0, %3, %c4\n\tbuffer_load_dwordx4 %5, %6, %0 offen " pol "lds" \
    : "=&s"(so) : "s"(lb), "i"(li), "s"(c), "i"(k), "v"(voff), "s"(r) : "memory", "scc")
#define DMA_ADD(pol, so, r, voff, lb, li, k) asm volatile("s_add_u32 m0, %1, %c2\n\ts_add_u32 %0, %0, %c3\n\tbuffer_load_dwordx4 %4, %5, %0 offen " pol "lds" \
    : "+s"(so) : "s"(lb), "i"(li), "i"(k), "v"(voff), "s"(r) : "memory", "scc")
#define PIN1(a) asm volatile("" : "+v"(a))
#define PIN2(a, b) asm volatile("" : "+v"(a), "+v"(b))
#define PIN3(a, b, c) asm volatile("" : "+v"(a), "+v"(b), "+v"(c))
#define SKEWN 0
#define SKEW() do { if (SKEWN) { u32 k_ = c16; asm volatile("s_cmp_eq_u32 %0, 0\n\ts_cbranch_scc1 2f\n1:\n\ts_nop %c1\n\ts_sub_u32 %0, %0, 1\n\ts_cmp_lg_u32 %0, 0\n\ts_cbranch_scc1 1b\n2:" : "+s"(k_) : "i"(SKEWN ? SKEWN - 1 : 0) : "scc"); } } while (0)
#define MK(s) asm volatile("; " s ::: "memory")
#define WAITV(n) asm volatile("s_waitcnt vmcnt(%c0)" ::"i"(n) : "memory")
#define ST128(v, r, voff, so) asm volatile("buffer_store_dwordx4 %0, %1, %2, %3 offen sc1\n\ts_nop 1" ::"v"(v), "v"(voff), "s"(r), "s"(so) : "memory")
#define ST128O(v, r, voff, so, o) asm volatile("buffer_store_dwordx4 %0, %1, %2, %3 offen offset:%c4 sc1\n\ts_nop 1" ::"v"(v), "v"(voff), "s"(r), "s"(so), "i"(o) : "memory")
#define ST64(v, r, voff, so) asm volatile("buffer_store_dwordx2 %0, %1, %2, %3 offen sc1" ::"v"(v), "v"(voff), "s"(r), "s"(so) : "memory")
#define ST32(v, r, voff, so) asm volatile("buffer_store_dword %0, %1, %2, %3 offen sc1" ::"v"(v), "v"(voff), "s"(r), "s"(so) : "memory")
#define LD128(d, r, voff) asm volatile("buffer_load_dwordx4 %0, %1, %2, 0 offen sc1" : "=v"(d) : "v"(voff), "s"(r) : "memory")
DI void bar() { asm volatile("s_waitcnt lgkmcnt(0)\n\ts_barrier" ::: "memory"); }
DI void sb() { __builtin_amdgcn_sched_barrier(0); }
DI void fence() { asm volatile("" ::: "memory"); sb(); }
DI void poll(const unsigned char* ws, u32 off, bool active, u64 tag) {
  for (u32 sp = 0; sp < (1u << 16); ++sp) {
    u64 v = tag;
    if (active) v = __hip_atomic_load((gu64*)(ws + off), __ATOMIC_RELAXED, __HIP_MEMORY_SCOPE_AGENT);
    if (__all(v == tag)) break;
  }
  fence();
}
#define OPTW(ww, x) do { if (w == ww) { asm volatile("; OPT_BEGIN"); x; asm volatile("; OPT_END"); } } while (0)
#define OPT(x) do { if (w == 0) { asm volatile("; OPT_BEGIN"); x; asm volatile("; OPT_END"); } } while (0)
#define SIGNAL(off) OPT(ST64(tagv, rWS, vF, (u32)(off)))
#define POLL_ISSUE(off) OPT(asm volatile("s_mov_b32 m0, %0\n\ts_nop 0\n\tbuffer_load_dword %1, %2, 0 offen sc1 lds" ::"s"(LC_PL), "v"((off) + (lane & 1) * 4), "s"(rWS) : "memory"))
#define POLL_CHECK(n, off, active) OPT(WAITV(n); const u32 v_ = lds_ld<u32>(LC_PL + lane * 4); \
    if (!__all(v_ == ((lane & 1) ? tagv.y : tagv.x) || !(active))) poll(ws, off, active, ((u64)tagv.y << 32) | tagv.x))
DI float fswap32_sum(float a, float b) { auto r = __builtin_amdgcn_permlane32_swap(__float_as_uint(a), __float_as_uint(b), false, false); return __uint_as_float(r[0]) + __uint_as_float(r[1]); }
DI float fswap16_sum(float a, float b) { auto r = __builtin_amdgcn_permlane16_swap(__float_as_uint(a), __float_as_uint(b), false, false); return __uint_as_float(r[0]) + __uint_as_float(r[1]); }
template <int CTRL> DI float dpp(float v) { return __int_as_float(__builtin_amdgcn_update_dpp(0, __float_as_int(v), CTRL, 0xf, 0xf, true)); }
DI float sum8(float v) {
  v += dpp<0xB1>(v);
  v += dpp<0x4E>(v);
  v += dpp<0x141>(v);
  return v;
}
struct cz { f32x2 z, n; };
DI cz mkz(f32x2 z) { return {z, (f32x2){-z.y, z.x}}; }
DI f32x2 cmul(f32x2 a, const cz& b) { return fma2(bc(a.y), b.n, bc(a.x) * b.z); }
DI f32x2 zpow(float a2, float b2, float r) {
  const float e = __builtin_amdgcn_exp2f(a2 * r), f = __builtin_amdgcn_fractf(b2 * r);
  return bc(e) * (f32x2){__builtin_amdgcn_cosf(f), __builtin_amdgcn_sinf(f)};
}
DI int wrap7(int v) { return v >= 7 ? v - 7 : v; }

#define STAMP(i)

template <int V> struct IC { static constexpr int v = V; };

__global__ void __launch_bounds__(512, 2)
s4mix_kernel(const u16* __restrict__ gx, const u16* __restrict__ gw1, unsigned char* __restrict__ ws, const u16* __restrict__ gw2,
             const u16* __restrict__ gw3, const u16* __restrict__ gw4, const u16* __restrict__ gAre, const u16* __restrict__ gAim,
             const u16* __restrict__ gBre, const u16* __restrict__ gBim, const u16* __restrict__ gCre, const u16* __restrict__ gCim,
             const u16* __restrict__ gLogdt, const u16* __restrict__ gDskip, u16* __restrict__ gout) {
  STAMP(0);
  const int tid = threadIdx.x, lane = tid & 63, n16 = lane & 15, g = lane >> 4;
  const int w = __builtin_amdgcn_readfirstlane(tid >> 6);
  const int bid = blockIdx.x, slot = bid >> 3;
  const int pq = (slot * 37) >> 8, j = slot - 7 * pq, p = 4 * (bid & 7) + pq;
  const u32 vW = opq((8 * w + (lane >> 3)) * 896 + 128 * j + (((lane & 7) ^ (((g & 1) | ((w & 1) << 1)) << 1)) << 4));
  const u32 vX = opq((64 * p + 8 * w + (lane >> 3)) * 896 + (((lane & 7) ^ ((4 * w + g) & 7)) << 4));
  const u32 lw = w * 1024;
  const int rot = wrap7(j + 2 * pq);
  const u32 pcc = (u32)(0x65432106543210ull >> (4 * rot));
  const u32 cc0 = rot, cc1 = (pcc >> 4) & 15;
  u32 so, su;
  const u16* const gP = ((const u16* const __attribute__((address_space(4)))*)__builtin_amdgcn_kernarg_segment_ptr())[6 + (w < 5 ? w : 5)];
  asm volatile("" ::"s"(gx), "s"(gw1));
  fence();
  if (slot >= 28) {
    const int hq = slot - 28;
    const u64 ht0 = __builtin_amdgcn_s_memrealtime();
#define HSTOP 100000
#define HLATE() (__builtin_amdgcn_s_memrealtime() - ht0 > HSTOP)
#define PT 0
#define HDELAY 2
    for (int i = 0; i < HDELAY; ++i) __builtin_amdgcn_s_sleep(16);
    const u32x4 r2 = mk_srd(gw2, D * D * 2), r3 = mk_srd(gw3, D * F * 2);
    asm volatile("s_mov_b32 m0, %0" ::"s"(LA));
#pragma unroll 1
    for (int i = 0; i < 2; ++i) {
      const int k = (2 * w + i) * 64 + lane;
      asm volatile("buffer_load_dword %0, %1, 0 offen lds" ::"v"(k < 784 ? (hq * 784 + k) * 128 : 0x7ffffff0u), "s"(r2) : "memory");
    }
    const u32x4 r4 = mk_srd(gw4, D * F * 2);
    {
      const u32 o = lane < 49 ? (((bid & 7) * 4 + hq) * 392 + w * 49 + lane) * 128 : 0x7ffffff0u;
      asm volatile("s_barrier\n\tbuffer_load_dword %0, %1, 0 offen lds\n\ts_barrier\n\tbuffer_load_dword %0, %2, 0 offen lds" ::"v"(o), "s"(r3), "s"(r4) : "memory");
    }
#define PRETOUCH(base, bytes, off0) do { const u32x4 rr = mk_srd(base, bytes); _Pragma("unroll 1") for (u32 o = (off0) + ((((bid & 7) * 4 + hq) * 8 + w) * 64 + lane) * 128; o - lane * 128 < (bytes); o += 256 * 8192) \
      asm volatile("buffer_load_dword %0, %1, 0 offen lds" ::"v"(o), "s"(rr) : "memory"); } while (0)
#pragma unroll 1
    for (int i = 0; i < 7 && !HLATE(); ++i)
      asm volatile("s_barrier\n\tbuffer_load_dword %0, %1, 0 offen lds" ::"v"(w < 7 ? (64 * wrap7(w + i) + 16 * hq + (lane >> 2)) * (F * 2) + (4 * w + (lane & 3)) * 128 : 0x7ffffff0u), "s"(r3) : "memory");
#pragma unroll 1
    for (int i = 0; i < 7 && !HLATE(); ++i)
      asm volatile("s_barrier\n\tbuffer_load_dword %0, %1, 0 offen lds" ::"v"(w < 7 ? (256 * wrap7(w + i) + 64 * hq + lane) * (D * 2) + 128 * w : 0x7ffffff0u), "s"(r4) : "memory");
    WAITV(0);
    return;
  }
  const u32x4 rX = mk_srd(gx, L * D * 2), rW1 = mk_srd(gw1, D * D * 2);
  fence();
  DMA_MUL("", so, rX, vX, lw, LA + 0, cc0, 128);
  fence();
  DMA_MUL("", so, rW1, vW, lw, LB + 0, cc0, 57344);
  fence();
  if (w == 7) {
    u64 pc; u32 len;
    asm volatile("s_getpc_b64 %0\n.Ls4_pc:\n\ts_mov_b32 %1, .Ls4_end-.Ls4_pc" : "=s"(pc), "=s"(len));
    const u32x4 rC = mk_srd((const void*)(pc & ~3ull), len & ~3u);
    asm volatile("s_mov_b32 m0, %0\n\ts_nop 0\n\tbuffer_load_dword %1, %2, 0 offen lds" ::"s"(LC_PF), "v"(g == 0 ? (slot * 16 + n16) * 128 : 0x7ffffff0u), "s"(rC) : "memory");
  }
  fence();
  asm volatile("s_barrier" ::: "memory");
  fence();
  DMA_MUL("", so, rW1, vW, lw, LB + 8192, cc1, 57344);
  fence();
  const int cl = w >> 2, c16 = w & 3, ch = 64 * j + 2 * p + cl;
  u32 scr = LC_SCR + w * 4352;
  asm volatile("s_add_u32 m0, %0, %c1\n\ts_nop 0\n\tglobal_load_lds_dwordx4 %2, %3" ::"s"(lw), "i"(LC_SCR), "v"((64 * j + 2 * p) * (NS * 2) + lane * 16), "s"(gP) : "memory", "scc");
  fence();
  __builtin_amdgcn_s_setprio(2);
  STAMP(1);
  const u32 ldw = ((const u32*)gLogdt)[ch >> 1], dsw = ((const u32*)gDskip)[ch >> 1];
  const u32x4 rWS = mk_srd(ws, WS_END), rW2 = mk_srd(gw2, D * D * 2), rW3 = mk_srd(gw3, D * F * 2), rW4 = mk_srd(gw4, D * F * 2);
  u32 cc2 = (pcc >> 8) & 15, cc3 = (pcc >> 12) & 15, cc4 = (pcc >> 16) & 15, cc5 = (pcc >> 20) & 15, cc6 = (pcc >> 24) & 15;
  const u32 pcj = (u32)(0x65432106543210ull >> (4 * j));
  u32 cj0 = j, cj1 = (pcj >> 4) & 15, cj2 = (pcj >> 8) & 15, cj3 = (pcj >> 12) & 15, cj4 = (pcj >> 16) & 15, cj5 = (pcj >> 20) & 15, cj6 = (pcj >> 24) & 15;
  asm volatile("" : "+s"(cc2), "+s"(cc3), "+s"(cc4), "+s"(cc5), "+s"(cc6), "+s"(cj1), "+s"(cj2), "+s"(cj3), "+s"(cj4), "+s"(cj5), "+s"(cj6));
  const int ta = w >> 2, tk = 16 * ta + n16;
  const u32 l16 = opq(lane * 16), q4 = n16 >> 2, l8 = (lane & 3) * 8, g1 = g & 1, g2 = g >> 1;
  const u32 aW = opq(LB + (8 * g + q4) * 128 + ((c16 ^ ((q4 >> 1) | (g1 << 1))) << 5) + l8);
  const u32 swx = (n16 >> 1) & 7;
  u32x4 ones = {0x3f803f80u, 0x3f803f80u, 0x3f803f80u, 0x3f803f80u};
  PIN1(ones);
  const int kgo = 8 * j + 2 * c16;
  u32 sHs = WS_H + kgo * HROW + (8 + 64 * p) * 16, sXs = sHs + (WS_X1 - WS_H);
  u32 hs0, hs1, hs2, hs3, hs4, hs5, hs6;
  {
    const u32 cj = (pcj >> (4 * (lane & 7))) & 15, qd = ((8 * cj + w) * 37) >> 9;
    const u32 v = cj * (8 * HROW) + __builtin_amdgcn_sbfe(0x20e010f0, 8 * qd, 8) + 32;
    hs0 = __builtin_amdgcn_readlane(v, 0); hs1 = __builtin_amdgcn_readlane(v, 1); hs2 = __builtin_amdgcn_readlane(v, 2); hs3 = __builtin_amdgcn_readlane(v, 3);
    hs4 = __builtin_amdgcn_readlane(v, 4); hs5 = __builtin_amdgcn_readlane(v, 5); hs6 = __builtin_amdgcn_readlane(v, 6);
    asm volatile("" : "+s"(hs0), "+s"(hs1), "+s"(hs2), "+s"(hs3), "+s"(hs4), "+s"(hs5), "+s"(hs6));
  }
  const u32 vO = opq((tk + 32 * g1) * 16 + g2 * HROW);
  const u32 vH = opq(l16 + WS_H + w * HROW + (6 + 64 * p) * 16);
  const int co = wrap7(7 - 2 * pq);
  const u32 aX0 = opq(LA + tk * 128 + ((g ^ swx) << 4)), aX1 = opq(aX0 ^ 64), aO = opq(LA + lw + (n16 & 7) * 128 + (((2 * g + (n16 >> 3)) ^ (n16 & 6)) << 4));
  const u32 ao = opq(LA + co * 8192 + tk * 128 + (((2 * c16 + g2) ^ swx) << 4) + g1 * 8);
  u32 au0, au1;
  {
    const u32 t0 = 8 * (4 * ta + g) + 7 - q4, pc = 2 * c16 + ((lane >> 1) & 1);
    au0 = opq(LA + co * 8192 + (lane & 1) * 8 + t0 * 128 + ((pc ^ ((t0 >> 1) & 7)) << 4)); au1 = opq((au0 ^ 32) - 512);
  }
  const u32 vU = opq(n16 * 4096 + g * 16);
  u32 sU = WS_U + (64 * j + 16 * c16) * 4096 + (8 * p + 4 * ta) * 16;
  const u32 scr_rd = opq(scr + n16 * 272 + g * 16);
  const u32 aPr = opq(LC_SCR + w * 128 + lane * 2);
  const u32 vF = opq(lane ? 0x7ffffff0u : 0u);
  const u32 vsw = ((lane & 3) ^ (g1 << 1)) << 4;
  const u32 v3 = opq((8 * g + q4) * (F * 2) + (256 * j + 32 * w) * 2 + vsw);
  u32 stg = LB + w * 7168, stc = LC_SCR + w * 4096;
  const u32 tb = opq(stg + (4 * g + q4) * 64 + (g1 << 5) + l8), tbx = opq(tb ^ 32), tA = opq(tb - 57344), tAx = opq(tbx - 57344);
  asm volatile("" : "+s"(sHs), "+s"(sXs), "+s"(sU), "+s"(stg), "+s"(stc), "+s"(scr));

  fence();
  DMA_MUL("", so, rX, vX, lw, LA + 8192, cc1, 128);
  fence();
  u32 ra0, ra1, ra2, ra3;
  {
    const u32 rec = LC_REC + cl * 4608 + n16 * 16, nul = LC_PF + lane * 4;
    if (c16 == 0) { ra0 = rec + g2 * 256 + g1 * 8; ra2 = ra0 + 4; ra1 = ra3 = nul; }
    else if (c16 == 1) { ra0 = rec + (g2 ? 2 * g : 2 + g) * 256; ra2 = ra0 + (g2 ? 256 : 4); ra1 = ra3 = nul; }
    else { ra0 = g < 3 ? rec + (8 + 4 * g) * 256 : nul; ra1 = g2 ? nul : ra0 + 256; ra2 = g2 ? nul : ra0 + 512; ra3 = g2 ? nul : ra0 + 768; }
    asm volatile("" : "+v"(ra0), "+v"(ra1), "+v"(ra2), "+v"(ra3));
  }
  const u32 rpa = opq(LC_SCR + cl * 4 * 4352 + c16 * 1024 + l16);
  const u32 LL = 0x01000100u, ID = 0x03020100u, NN = 0x05040504u, HL = 0x05040302u;
  const bool r0 = c16 == 0 && lane < 32, r1 = c16 == 0;
  u32 e0, e1, e2, e3, e4, e5;
  if (c16 == 0) {
    e0 = r0 ? ID : LL; e1 = r0 ? 0x07060504u : HL; e2 = e0; e3 = r0 ? ID : HL;
    e4 = lane < 31 ? ID : LL; e5 = lane < 31 ? 0x07060504u : lane < 63 ? HL : ID;
  } else if (c16 == 1) { e0 = LL; e1 = ID; e2 = 0x03020302u; e3 = NN; e4 = LL; e5 = lane < 63 ? ID : LL; }
  else { e0 = e1 = e4 = e5 = LL; e2 = (lane & 1) ? NN : 0x05040100u; e3 = NN; }
  asm volatile("" : "+v"(e0), "+v"(e1), "+v"(e2), "+v"(e3), "+v"(e4), "+v"(e5));
  const u32 rra = opq(LC_REC + cl * 4608 + c16 * 1024 + l16), cp = opq(LC_SCR + cl * 16640 + c16 * 1024 + l16);
  const u32 aA = opq(LC_SCR + cl * 16640 + c16 * 128 + (n16 & 3) * 4160 + ((n16 & 12) + 8 * g) * 2);
  const u32 aB = opq(LC_REC + cl * 4608 - c16 * 128 + (2 * n16 + 29 - g) * 16);
  const u32 aX = opq(LA + g * 1024 + n16 * 16);
  const u32 aSt = opq(LC_STA + n16 * 16);
  float dt = rbf(expf(cl ? bf_hi(ldw) : bf_lo(ldw)));
  asm volatile("" : "+v"(dt));
  f32x4 acc1[2] = {}, cs1 = {};
  f32x4 S1 = {}, S2 = {};
  u32x4 fw[2][2], fx[2][2][2], fo[2];
  fence();
  WAITV(1);
  fence();
  asm volatile("s_barrier" ::: "memory");
  fence();
  STAMP(24);
  fence();
  if (ta == 0) { u32 so;
  fence();
  DMA_MUL("", so, rX, vX, lw, LA + 16384, cc2, 128);
  fence();
  DMA_MUL("", so, rW1, vW, lw, LB + 16384, cc2, 57344);
  fence();
  DMA_MUL("", so, rX, vX, lw, LA + 24576, cc3, 128);
  fence();
  DMA_MUL("", so, rW1, vW, lw, LB + 24576, cc3, 57344);
  fence();
  }
  fence();
  fw[0][0] = lds_tr2(aW + 0, aW + 512);
  fence();
  fx[0][0][0] = lds_ld<u32x4>(aX0 + 0);
  fence();
  fx[0][0][1] = lds_ld<u32x4>(aX0 + 4096);
  fence();
  fw[0][1] = lds_tr2(aW + 4096, aW + 4608);
  fence();
  fx[0][1][0] = lds_ld<u32x4>(aX1 + 0);
  fence();
  fx[0][1][1] = lds_ld<u32x4>(aX1 + 4096);
  fence();
  fo[0] = lds_ld<u32x4>(aO + 0);
  fence();
  if (ta == 1) { u32 so;
  fence();
  DMA_MUL("", so, rX, vX, lw, LA + 16384, cc2, 128);
  fence();
  DMA_MUL("", so, rW1, vW, lw, LB + 16384, cc2, 57344);
  fence();
  DMA_MUL("", so, rX, vX, lw, LA + 24576, cc3, 128);
  fence();
  DMA_MUL("", so, rW1, vW, lw, LB + 24576, cc3, 57344);
  fence();
  }
  fence();
  WAITV(4);
  fence();
  asm volatile("s_barrier" ::: "memory");
  fence();
  STAMP(25);
  fence();
  if (ta == 0) { u32 so;
  fence();
  DMA_MUL("", so, rX, vX, lw, LA + 32768, cc4, 128);
  fence();
  DMA_MUL("", so, rX, vX, lw, LA + 40960, cc5, 128);
  fence();
  DMA_MUL("", so, rW1, vW, lw, LB + 32768, cc4, 57344);
  fence();
  }
  fence();
  fw[1][0] = lds_tr2(aW + 8192, aW + 8704);
  fence();
  acc1[0] = mfma_bf(fw[0][0], fx[0][0][0], acc1[0]); PIN1(acc1[0]);
  fence();
  fx[1][0][0] = lds_ld<u32x4>(aX0 + 8192);
  fence();
  acc1[1] = mfma_bf(fw[0][0], fx[0][0][1], acc1[1]); PIN1(acc1[1]);
  fence();
  fx[1][0][1] = lds_ld<u32x4>(aX0 + 12288);
  fence();
  cs1 = mfma_bf(fw[0][0], ones, cs1); PIN1(cs1);
  fence();
  fw[1][1] = lds_tr2(aW + 12288, aW + 12800);
  fence();
  S1 = mfma_bf(ones, fo[0], S1); S2 = mfma_bf(fo[0], fo[0], S2); PIN2(S1, S2);
  fence();
  fo[1] = lds_ld<u32x4>(aO + 8192);
  fence();
  acc1[0] = mfma_bf(fw[0][1], fx[0][1][0], acc1[0]); PIN1(acc1[0]);
  fence();
  fx[1][1][0] = lds_ld<u32x4>(aX1 + 8192);
  fence();
  acc1[1] = mfma_bf(fw[0][1], fx[0][1][1], acc1[1]); PIN1(acc1[1]);
  fence();
  fx[1][1][1] = lds_ld<u32x4>(aX1 + 12288);
  fence();
  cs1 = mfma_bf(fw[0][1], ones, cs1); PIN1(cs1);
  fence();
  if (ta == 1) { u32 so;
  fence();
  DMA_MUL("", so, rX, vX, lw, LA + 32768, cc4, 128);
  fence();
  DMA_MUL("", so, rX, vX, lw, LA + 40960, cc5, 128);
  fence();
  DMA_MUL("", so, rW1, vW, lw, LB + 32768, cc4, 57344);
  fence();
  }
  fence();
  WAITV(5);
  fence();
  asm volatile("s_barrier" ::: "memory");
  fence();
  STAMP(26);
  fence();
  if (ta == 0) { u32 so;
  fence();
  DMA_MUL("", so, rX, vX, lw, LA + 49152, cc6, 128);
  fence();
  DMA_MUL("", so, rW1, vW, lw, LB + 40960, cc5, 57344);
  fence();
  }
  fence();
  fw[0][0] = lds_tr2(aW + 16384, aW + 16896);
  fence();
  acc1[0] = mfma_bf(fw[1][0], fx[1][0][0], acc1[0]); PIN1(acc1[0]);
  fence();
  fx[0][0][0] = lds_ld<u32x4>(aX0 + 16384);
  fence();
  acc1[1] = mfma_bf(fw[1][0], fx[1][0][1], acc1[1]); PIN1(acc1[1]);
  fence();
  fx[0][0][1] = lds_ld<u32x4>(aX0 + 20480);
  fence();
  cs1 = mfma_bf(fw[1][0], ones, cs1); PIN1(cs1);
  fence();
  fw[0][1] = lds_tr2(aW + 20480, aW + 20992);
  fence();
  S1 = mfma_bf(ones, fo[1], S1); S2 = mfma_bf(fo[1], fo[1], S2); PIN2(S1, S2);
  fence();
  fo[0] = lds_ld<u32x4>(aO + 16384);
  fence();
  acc1[0] = mfma_bf(fw[1][1], fx[1][1][0], acc1[0]); PIN1(acc1[0]);
  fence();
  fx[0][1][0] = lds_ld<u32x4>(aX1 + 16384);
  fence();
  acc1[1] = mfma_bf(fw[1][1], fx[1][1][1], acc1[1]); PIN1(acc1[1]);
  fence();
  fx[0][1][1] = lds_ld<u32x4>(aX1 + 20480);
  fence();
  cs1 = mfma_bf(fw[1][1], ones, cs1); PIN1(cs1);
  fence();
  if (ta == 1) { u32 so;
  fence();
  DMA_MUL("", so, rX, vX, lw, LA + 49152, cc6, 128);
  fence();
  DMA_MUL("", so, rW1, vW, lw, LB + 40960, cc5, 57344);
  fence();
  }
  fence();
  WAITV(5);
  fence();
  asm volatile("s_barrier" ::: "memory");
  fence();
  STAMP(27);
  fence();
  if (ta == 0) { u32 so;
  fence();
  DMA_MUL("", so, rW1, vW, lw, LB + 49152, cc6, 57344);
  fence();
  }
  fence();
  fw[1][0] = lds_tr2(aW + 24576, aW + 25088);
  fence();
  acc1[0] = mfma_bf(fw[0][0], fx[0][0][0], acc1[0]); PIN1(acc1[0]);
  fence();
  fx[1][0][0] = lds_ld<u32x4>(aX0 + 24576);
  fence();
  acc1[1] = mfma_bf(fw[0][0], fx[0][0][1], acc1[1]); PIN1(acc1[1]);
  fence();
  fx[1][0][1] = lds_ld<u32x4>(aX0 + 28672);
  fence();
  cs1 = mfma_bf(fw[0][0], ones, cs1); PIN1(cs1);
  fence();
  fw[1][1] = lds_tr2(aW + 28672, aW + 29184);
  fence();
  S1 = mfma_bf(ones, fo[0], S1); S2 = mfma_bf(fo[0], fo[0], S2); PIN2(S1, S2);
  fence();
  fo[1] = lds_ld<u32x4>(aO + 24576);
  fence();
  acc1[0] = mfma_bf(fw[0][1], fx[0][1][0], acc1[0]); PIN1(acc1[0]);
  fence();
  fx[1][1][0] = lds_ld<u32x4>(aX1 + 24576);
  fence();
  acc1[1] = mfma_bf(fw[0][1], fx[0][1][1], acc1[1]); PIN1(acc1[1]);
  fence();
  fx[1][1][1] = lds_ld<u32x4>(aX1 + 28672);
  fence();
  cs1 = mfma_bf(fw[0][1], ones, cs1); PIN1(cs1);
  fence();
  if (ta == 1) { u32 so;
  fence();
  DMA_MUL("", so, rW1, vW, lw, LB + 49152, cc6, 57344);
  fence();
  }
  fence();
  OPT(asm volatile("s_mov_b32 m0, %0\n\ts_nop 0\n\tbuffer_load_dword %1, %2, 0 offen sc1 lds" ::"s"(LC_E), "v"(lane * 4), "s"(rWS) : "memory"));
  fence();
  WAITV(3);
  fence();
  asm volatile("s_barrier" ::: "memory");
  fence();
  STAMP(28);
  fo[0] = lds_ld<u32x4>(aO + 32768);
  fence();
  fw[0][0] = lds_tr2(aW + 32768, aW + 33280);
  fence();
  acc1[0] = mfma_bf(fw[1][0], fx[1][0][0], acc1[0]); PIN1(acc1[0]);
  fence();
  fx[0][0][0] = lds_ld<u32x4>(aX0 + 32768);
  fence();
  acc1[1] = mfma_bf(fw[1][0], fx[1][0][1], acc1[1]); PIN1(acc1[1]);
  fence();
  fx[0][0][1] = lds_ld<u32x4>(aX0 + 36864);
  fence();
  cs1 = mfma_bf(fw[1][0], ones, cs1); PIN1(cs1);
  fence();
  fw[0][1] = lds_tr2(aW + 36864, aW + 37376);
  fence();
  S1 = mfma_bf(ones, fo[1], S1); S2 = mfma_bf(fo[1], fo[1], S2); PIN2(S1, S2);
  fence();
  fo[1] = lds_ld<u32x4>(aO + 40960);
  fence();
  acc1[0] = mfma_bf(fw[1][1], fx[1][1][0], acc1[0]); PIN1(acc1[0]);
  fence();
  fx[0][1][0] = lds_ld<u32x4>(aX1 + 32768);
  fence();
  acc1[1] = mfma_bf(fw[1][1], fx[1][1][1], acc1[1]); PIN1(acc1[1]);
  fence();
  fx[0][1][1] = lds_ld<u32x4>(aX1 + 36864);
  fence();
  cs1 = mfma_bf(fw[1][1], ones, cs1); PIN1(cs1);
  fence();
  S1 = mfma_bf(ones, fo[0], S1); S2 = mfma_bf(fo[0], fo[0], S2); PIN2(S1, S2);
  fence();
  WAITV(1);
  fence();
  asm volatile("s_barrier" ::: "memory");
  fence();
  STAMP(29);
  fo[0] = lds_ld<u32x4>(aO + 49152);
  fence();
  fw[1][0] = lds_tr2(aW + 40960, aW + 41472);
  fence();
  S1 = mfma_bf(ones, fo[1], S1); S2 = mfma_bf(fo[1], fo[1], S2); PIN2(S1, S2);
  fence();
  fx[1][0][0] = lds_ld<u32x4>(aX0 + 40960);
  fence();
  fx[1][0][1] = lds_ld<u32x4>(aX0 + 45056);
  fence();
  S1 = mfma_bf(ones, fo[0], S1); S2 = mfma_bf(fo[0], fo[0], S2); PIN2(S1, S2);
  fence();
  fw[1][1] = lds_tr2(aW + 45056, aW + 45568);
  fence();
  acc1[0] = mfma_bf(fw[0][0], fx[0][0][0], acc1[0]); PIN1(acc1[0]);
  fence();
  fx[1][1][0] = lds_ld<u32x4>(aX1 + 40960);
  fence();
  acc1[1] = mfma_bf(fw[0][0], fx[0][0][1], acc1[1]); PIN1(acc1[1]);
  fence();
  fx[1][1][1] = lds_ld<u32x4>(aX1 + 45056);
  fence();
  cs1 = mfma_bf(fw[0][0], ones, cs1); PIN1(cs1);
  fence();
  float s1 = S1.x, s2 = (n16 & 2) ? ((n16 & 1) ? S2.w : S2.z) : ((n16 & 1) ? S2.y : S2.x);
  s2 = g == q4 ? s2 : 0.f;
  s2 = fswap32_sum(s2, s2); s2 = fswap16_sum(s2, s2);
  s1 += dpp<0x128>(s1); s2 += dpp<0x128>(s2);
  const float mu1 = s1 * (1.f / D), var1 = fmaxf(fnma1(mu1, mu1, s2 * (1.f / D)), 0.f);
  const float mb1 = rbf(mu1), sd1 = rbf(__builtin_amdgcn_sqrtf(rbf(rbf(var1) + 1e-5f)));
  acc1[0] = mfma_bf(fw[0][1], fx[0][1][0], acc1[0]); PIN1(acc1[0]);
  fence();
  acc1[1] = mfma_bf(fw[0][1], fx[0][1][1], acc1[1]); PIN1(acc1[1]);
  fence();
  cs1 = mfma_bf(fw[0][1], ones, cs1); PIN1(cs1);
  fence();
  WAITV(0);
  fence();
  asm volatile("s_barrier" ::: "memory");
  fence();
  STAMP(30);
  fw[0][0] = lds_tr2(aW + 49152, aW + 49664);
  fence();
  fx[0][0][0] = lds_ld<u32x4>(aX0 + 49152);
  fence();
  fx[0][0][1] = lds_ld<u32x4>(aX0 + 53248);
  fence();
  acc1[0] = mfma_bf(fw[1][0], fx[1][0][0], acc1[0]); PIN1(acc1[0]);
  fence();
  fw[0][1] = lds_tr2(aW + 53248, aW + 53760);
  fence();
  acc1[1] = mfma_bf(fw[1][0], fx[1][0][1], acc1[1]); PIN1(acc1[1]);
  fence();
  fx[0][1][0] = lds_ld<u32x4>(aX1 + 49152);
  fence();
  cs1 = mfma_bf(fw[1][0], ones, cs1); PIN1(cs1);
  fence();
  fx[0][1][1] = lds_ld<u32x4>(aX1 + 53248);
  fence();
  u32x2 xr[2], xn[2];
  xr[0] = lds_ld<u32x2>(ao); xr[1] = lds_ld<u32x2>(ao + 4096);
  {
    const float isd = __builtin_amdgcn_rcpf(sd1);
    const float m2 = rbf((mu1 - mb1) * isd), sd2 = rbf(__builtin_amdgcn_sqrtf(rbf(rbf(mul1t(var1 * isd, isd)) + 1e-5f)));
    const float rs = isd * __builtin_amdgcn_rcpf(sd2);
    if (lane < 8) {
      lds_st<f32x4>(LC_STA + w * 128 + l16, (f32x4){fma1(m2, sd1, mb1), GC1 * rs, mul1(sd1, sd2), mul1(GC2 * rs, rs * rs)});
      lds_st<f32x2>(LC_STB + w * 64 + lane * 8, (f32x2){mb1, isd});
    }
  }
  acc1[0] = mfma_bf(fw[1][1], fx[1][1][0], acc1[0]); PIN1(acc1[0]);
  fence();
  acc1[1] = mfma_bf(fw[1][1], fx[1][1][1], acc1[1]); PIN1(acc1[1]);
  fence();
  cs1 = mfma_bf(fw[1][1], ones, cs1); PIN1(cs1);
  fence();
  bar();
  fence();
  const f32x4 k10 = lds_ld<f32x4>(aSt + ta * 256), k11 = lds_ld<f32x4>(aSt + ta * 256 + 512);
  acc1[0] = mfma_bf(fw[0][0], fx[0][0][0], acc1[0]); PIN1(acc1[0]);
  fence();
  acc1[1] = mfma_bf(fw[0][0], fx[0][0][1], acc1[1]); PIN1(acc1[1]);
  fence();
  cs1 = mfma_bf(fw[0][0], ones, cs1); PIN1(cs1);
  fence();
  acc1[0] = mfma_bf(fw[0][1], fx[0][1][0], acc1[0]); PIN1(acc1[0]);
  fence();
  acc1[1] = mfma_bf(fw[0][1], fx[0][1][1], acc1[1]); PIN1(acc1[1]);
  fence();
  cs1 = mfma_bf(fw[0][1], ones, cs1); PIN1(cs1);
  fence();
  STAMP(2);
  {
    auto q0 = __builtin_amdgcn_permlane16_swap(pk_bf(gelu2(lo2(acc1[0]), lo2(cs1), k10)), pk_bf(gelu2(lo2(acc1[1]), lo2(cs1), k11)), false, false);
    auto q1 = __builtin_amdgcn_permlane16_swap(pk_bf(gelu2(hi2(acc1[0]), hi2(cs1), k10)), pk_bf(gelu2(hi2(acc1[1]), hi2(cs1), k11)), false, false);
    if (p == 0 || p == 31) {
      const u32x4 z = {0, 0, 0, 0};
      ST128(z, rWS, lane < 8 ? l16 : 0x7ffffff0u, WS_H + (8 * j + w) * HROW + (p ? 2056 * 16 : 0));
    }
    const u32x4 hv = {q0[0], q1[0], q0[1], q1[1]};
    ST128(hv, rWS, vO, sHs);
  }
  __builtin_amdgcn_s_setprio(0);
  fence();
  STAMP(31);
  u32 prm[6];
  const f32x2 st0 = lds_ld<f32x2>(LC_STB + tk * 8), st1 = lds_ld<f32x2>(LC_STB + tk * 8 + 256);
  _Pragma("unroll") for (int i = 0; i < 6; ++i) prm[i] = lds_ld<u16>(aPr + i * 1024);
  u32x2 tagv;
  { const u64 t = lds_ld<u64>(LC_E) + 1; tagv = (u32x2){(u32)t, (u32)(t >> 32)}; }
  fence();
  DMA_MUL("", so, rW2, vW, lw, LB + 0, cj0, 57344);
  fence();
  xn[0].x = pk_bf(bf2(pk_bf(bf2(xr[0].x) - bc(st0.x))) * bc(st0.y));
  xn[0].y = pk_bf(bf2(pk_bf(bf2(xr[0].y) - bc(st0.x))) * bc(st0.y));
  lds_st<u32x2>(ao, xn[0]);
  fence();
  DMA_MUL("", so, rW2, vW, lw, LB + 8192, cj1, 57344);
  fence();
  xn[1].x = pk_bf(bf2(pk_bf(bf2(xr[1].x) - bc(st1.x))) * bc(st1.y));
  xn[1].y = pk_bf(bf2(pk_bf(bf2(xr[1].y) - bc(st1.x))) * bc(st1.y));
  lds_st<u32x2>(ao + 4096, xn[1]);
  fence();
  DMA_MUL("", so, rW2, vW, lw, LB + 16384, cj2, 57344);
  fence();
  u32x4 W3f[14][2], W4f[7][4], W2f[14], Q[4], pf[4], pg[4];
  f32x4 cs3[2] = {}, kt[3] = {};
#define KW(row, val) kw[row] = pk_h(val)
  const float ka2 = dt * bf_lo(prm[0]) * 1.4426950408889634f, kb2 = dt * bf_lo(prm[1]) * 0.15915494309189535f;
  f32x2 wz = cmul((f32x2){bf_lo(prm[4]), -bf_lo(prm[5])}, mkz((f32x2){dt * bf_lo(prm[2]), -dt * bf_lo(prm[3])}));
  const f32x2 z8 = zpow(ka2, kb2, 8.f);
  fence();
  DMA_MUL("", so, rW2, vW, lw, LB + 24576, cj3, 57344);
  fence();
  WAITV(4);
  fence();
  bar();
  fence();
  SIGNAL(WS_FH + p * 128 + j * 8);
  fence();
  STAMP(3);
  const u32x4 uv = lds_tr2(au0, au1);
  const cz z16 = mkz(cmul(z8, mkz(z8)));
  float kinv;
  {
    float sw = sum8(fabsf(wz.x) + fabsf(wz.y));
    sw += dpp<0x140>(sw);
    sw = fswap32_sum(sw, sw); sw = fswap16_sum(sw, sw);
    u32 ex = __float_as_uint(sw) >> 23; ex = ex < 14 ? 14 : ex;
    kinv = __uint_as_float((ex - 13u) << 23);
    wz *= bc(__uint_as_float((267u - ex) << 23));
    if (lane == 0) lds_st<float>(LC_SW + w * 4, sw);
  }
  ST128(uv, rWS, vU, sU);
  fence();
  u32 kw[16];
  f32x2 qa = z16.z, qb = z8;
  kw[0] = 0x00003c00u;
  fence();
  DMA_MUL("", so, rW2, vW, lw, LB + 32768, cj4, 57344);
  fence();
  KW(1, qb); KW(2, qa); qb = cmul(qb, z16); qa = cmul(qa, z16);
  KW(3, qb); KW(4, qa); qb = cmul(qb, z16); qa = cmul(qa, z16);
  KW(5, qb); KW(6, qa); qb = cmul(qb, z16); qa = cmul(qa, z16);
  KW(7, qb); KW(8, qa); qb = cmul(qb, z16); qa = cmul(qa, z16);
  fence();
  DMA_MUL("", so, rW2, vW, lw, LB + 40960, cj5, 57344);
  fence();
  KW(9, qb); KW(10, qa); qb = cmul(qb, z16); qa = cmul(qa, z16);
  KW(11, qb); KW(12, qa); qb = cmul(qb, z16); qa = cmul(qa, z16);
  KW(13, qb); KW(14, qa); qb = cmul(qb, z16);
  KW(15, qb);
  cz z1 = mkz(zpow(ka2, -kb2, 1.f)), z128 = mkz(zpow(ka2, -kb2, 128.f));
  PIN2(z1.z, z128.z);
  fence();
  asm volatile("s_mov_b32 m0, %0\n\ts_nop 0" ::"s"(scr) : "memory");
  asm volatile("ds_write_addtid_b32 %0 offset:0\n\tds_write_addtid_b32 %1 offset:272\n\tds_write_addtid_b32 %2 offset:544\n\tds_write_addtid_b32 %3 offset:816" ::"v"(kw[0]), "v"(kw[1]), "v"(kw[2]), "v"(kw[3]) : "memory");
  asm volatile("ds_write_addtid_b32 %0 offset:1088\n\tds_write_addtid_b32 %1 offset:1360\n\tds_write_addtid_b32 %2 offset:1632\n\tds_write_addtid_b32 %3 offset:1904" ::"v"(kw[4]), "v"(kw[5]), "v"(kw[6]), "v"(kw[7]) : "memory");
  asm volatile("ds_write_addtid_b32 %0 offset:2176\n\tds_write_addtid_b32 %1 offset:2448\n\tds_write_addtid_b32 %2 offset:2720\n\tds_write_addtid_b32 %3 offset:2992" ::"v"(kw[8]), "v"(kw[9]), "v"(kw[10]), "v"(kw[11]) : "memory");
  asm volatile("ds_write_addtid_b32 %0 offset:3264\n\tds_write_addtid_b32 %1 offset:3536\n\tds_write_addtid_b32 %2 offset:3808\n\tds_write_addtid_b32 %3 offset:4080" ::"v"(kw[12]), "v"(kw[13]), "v"(kw[14]), "v"(kw[15]) : "memory");
  fence();
  _Pragma("unroll") for (int s = 0; s < 4; ++s) Q[s] = lds_ld<u32x4>(scr_rd + s * 64);
  fence();
  DMA_MUL("", so, rW2, vW, lw, LB + 49152, cj6, 57344);
  fence();
  f32x2 pa = wz, pb = cmul(wz, z128);
  wz = cmul(pb, z128);
  KW(0, pa); KW(8, pb); pa = cmul(pa, z1); pb = cmul(pb, z1);
  KW(1, pa); KW(9, pb); pa = cmul(pa, z1); pb = cmul(pb, z1);
  KW(2, pa); KW(10, pb); pa = cmul(pa, z1); pb = cmul(pb, z1);
  KW(3, pa); KW(11, pb); pa = cmul(pa, z1); pb = cmul(pb, z1);
  KW(4, pa); KW(12, pb); pa = cmul(pa, z1); pb = cmul(pb, z1);
  KW(5, pa); KW(13, pb); pa = cmul(pa, z1); pb = cmul(pb, z1);
  KW(6, pa); KW(14, pb); pa = cmul(pa, z1); pb = cmul(pb, z1);
  KW(7, pa); KW(15, pb);
  fence();
  POLL_ISSUE((((lane & 14) != 14 && (u32)(p - 1 + g) < 32u && lane < 48) ? WS_FH + (p - 1 + g) * 128 + ((lane >> 1) & 7) * 8 : 0x7ffffff0u));
  fence();
  asm volatile("s_mov_b32 m0, %0\n\ts_nop 0" ::"s"(scr) : "memory");
  asm volatile("ds_write_addtid_b32 %0 offset:0\n\tds_write_addtid_b32 %1 offset:272\n\tds_write_addtid_b32 %2 offset:544\n\tds_write_addtid_b32 %3 offset:816" ::"v"(kw[0]), "v"(kw[1]), "v"(kw[2]), "v"(kw[3]) : "memory");
  asm volatile("ds_write_addtid_b32 %0 offset:1088\n\tds_write_addtid_b32 %1 offset:1360\n\tds_write_addtid_b32 %2 offset:1632\n\tds_write_addtid_b32 %3 offset:1904" ::"v"(kw[4]), "v"(kw[5]), "v"(kw[6]), "v"(kw[7]) : "memory");
  asm volatile("ds_write_addtid_b32 %0 offset:2176\n\tds_write_addtid_b32 %1 offset:2448\n\tds_write_addtid_b32 %2 offset:2720\n\tds_write_addtid_b32 %3 offset:2992" ::"v"(kw[8]), "v"(kw[9]), "v"(kw[10]), "v"(kw[11]) : "memory");
  asm volatile("ds_write_addtid_b32 %0 offset:3264\n\tds_write_addtid_b32 %1 offset:3536\n\tds_write_addtid_b32 %2 offset:3808\n\tds_write_addtid_b32 %3 offset:4080" ::"v"(kw[12]), "v"(kw[13]), "v"(kw[14]), "v"(kw[15]) : "memory");
  fence();
  _Pragma("unroll") for (int s = 0; s < 4; ++s) pf[s] = lds_ld<u32x4>(scr_rd + s * 64);
  fence();
  const cz z2 = mkz(cmul(z1.z, z1)), z4 = mkz(cmul(z2.z, z2));
  pa = wz; pb = cmul(wz, z128);
  wz = cmul(pb, z128);
  KW(0, pa); KW(4, pb); pa = cmul(pa, z2); pb = cmul(pb, z2);
  KW(1, pa); KW(5, pb); pa = cmul(pa, z2); pb = cmul(pb, z2);
  KW(2, pa); KW(6, pb); pa = cmul(pa, z2); pb = cmul(pb, z2);
  KW(3, pa); KW(7, pb);
  KW(8, wz); KW(9, cmul(wz, z4)); wz = cmul(wz, z128);
  KW(10, wz); KW(11, cmul(wz, z4)); wz = cmul(wz, z128);
  KW(12, wz); KW(13, cmul(wz, z4)); wz = cmul(wz, z128);
  KW(14, wz); KW(15, cmul(wz, z4)); wz = cmul(wz, z128);
  fence();
  asm volatile("s_mov_b32 m0, %0\n\ts_nop 0" ::"s"(scr) : "memory");
  asm volatile("ds_write_addtid_b32 %0 offset:0\n\tds_write_addtid_b32 %1 offset:272\n\tds_write_addtid_b32 %2 offset:544\n\tds_write_addtid_b32 %3 offset:816" ::"v"(kw[0]), "v"(kw[1]), "v"(kw[2]), "v"(kw[3]) : "memory");
  asm volatile("ds_write_addtid_b32 %0 offset:1088\n\tds_write_addtid_b32 %1 offset:1360\n\tds_write_addtid_b32 %2 offset:1632\n\tds_write_addtid_b32 %3 offset:1904" ::"v"(kw[4]), "v"(kw[5]), "v"(kw[6]), "v"(kw[7]) : "memory");
  asm volatile("ds_write_addtid_b32 %0 offset:2176\n\tds_write_addtid_b32 %1 offset:2448\n\tds_write_addtid_b32 %2 offset:2720\n\tds_write_addtid_b32 %3 offset:2992" ::"v"(kw[8]), "v"(kw[9]), "v"(kw[10]), "v"(kw[11]) : "memory");
  asm volatile("ds_write_addtid_b32 %0 offset:3264\n\tds_write_addtid_b32 %1 offset:3536\n\tds_write_addtid_b32 %2 offset:3808\n\tds_write_addtid_b32 %3 offset:4080" ::"v"(kw[12]), "v"(kw[13]), "v"(kw[14]), "v"(kw[15]) : "memory");
  fence();
  _Pragma("unroll") for (int s = 0; s < 4; ++s) pg[s] = lds_ld<u32x4>(scr_rd + s * 64);
  fence();
  STAMP(4);
  WAITV(0);
  fence();
  POLL_CHECK(0, WS_FH + (p - 1 + g) * 128 + ((lane >> 1) & 7) * 8, ((lane & 14) != 14 && (u32)(p - 1 + g) < 32u && lane < 48));
  fence();
  bar();
  fence();
  SIGNAL(WS_FU + j * 256 + p * 8);
  fence();
  STAMP(5);
  fence();
  DMA_ADD("sc1 ", hs0, rWS, vH, lw, LA + 0, 0);
  fence();
  const cz z256 = mkz(cmul(z128.z, z128));
  f32x2 wb = cmul(wz, z128);
  KW(0, wz); KW(1, wb); wz = cmul(wz, z256); wb = cmul(wb, z256);
  KW(2, wz); KW(3, wb); wz = cmul(wz, z256); wb = cmul(wb, z256);
  KW(4, wz); KW(5, wb); wz = cmul(wz, z256); wb = cmul(wb, z256);
  KW(6, wz); KW(7, wb); wz = cmul(wz, z256);
  KW(8, wz);
  fence();
  asm volatile("s_mov_b32 m0, %0\n\ts_nop 0" ::"s"(scr) : "memory");
  asm volatile("ds_write_addtid_b32 %0 offset:0\n\tds_write_addtid_b32 %1 offset:272\n\tds_write_addtid_b32 %2 offset:544\n\tds_write_addtid_b32 %3 offset:816" ::"v"(kw[0]), "v"(kw[1]), "v"(kw[2]), "v"(kw[3]) : "memory");
  asm volatile("ds_write_addtid_b32 %0 offset:1088\n\tds_write_addtid_b32 %1 offset:1360\n\tds_write_addtid_b32 %2 offset:1632\n\tds_write_addtid_b32 %3 offset:1904" ::"v"(kw[4]), "v"(kw[5]), "v"(kw[6]), "v"(kw[7]) : "memory");
  asm volatile("ds_write_addtid_b32 %0 offset:2176" ::"v"(kw[8]) : "memory");
  fence();
  _Pragma("unroll") for (int s = 0; s < 4; ++s) kt[0] = mfma_h(pf[s], Q[s], kt[0]);
  PIN1(kt[0]);
  _Pragma("unroll") for (int s = 0; s < 4; ++s) pf[s] = lds_ld<u32x4>(scr_rd + s * 64);
  fence();
  DMA_ADD("sc1 ", hs1, rWS, vH, lw, LA + 8192, 0);
  fence();
  W2f[0] = lds_tr2(aW + 0, aW + 512);
  W2f[1] = lds_tr2(aW + 4096, aW + 4608);
  W2f[2] = lds_tr2(aW + 8192, aW + 8704);
  W2f[3] = lds_tr2(aW + 12288, aW + 12800);
  fence();
  DMA_ADD("sc1 ", hs2, rWS, vH, lw, LA + 16384, 0);
  fence();
  _Pragma("unroll") for (int s = 0; s < 4; ++s) kt[1] = mfma_h(pg[s], Q[s], kt[1]);
  PIN1(kt[1]);
  W2f[4] = lds_tr2(aW + 16384, aW + 16896);
  W2f[5] = lds_tr2(aW + 20480, aW + 20992);
  W2f[6] = lds_tr2(aW + 24576, aW + 25088);
  W2f[7] = lds_tr2(aW + 28672, aW + 29184);
  fence();
  DMA_ADD("sc1 ", hs3, rWS, vH, lw, LA + 24576, 0);
  fence();
  _Pragma("unroll") for (int s = 0; s < 4; ++s) kt[2] = mfma_h(pf[s], Q[s], kt[2]);
  PIN1(kt[2]);
  W2f[8] = lds_tr2(aW + 32768, aW + 33280);
  W2f[9] = lds_tr2(aW + 36864, aW + 37376);
  W2f[10] = lds_tr2(aW + 40960, aW + 41472);
  W2f[11] = lds_tr2(aW + 45056, aW + 45568);
  W2f[12] = lds_tr2(aW + 49152, aW + 49664);
  W2f[13] = lds_tr2(aW + 53248, aW + 53760);
  fence();
  _Pragma("unroll") for (int t = 0; t < 3; ++t) lds_st<f32x4>(scr + t * 1024 + l16, kt[t] * kinv);
  fence();
  DMA_ADD("sc1 ", hs4, rWS, vH, lw, LA + 32768, 0);
  fence();
  STAMP(32);
  fence();
  bar();
  fence();
  float yinv, ysc;
  {
    const f32x4 sv = lds_ld<f32x4>(LC_SW + cl * 16);
    u32 ex = __float_as_uint(add1(sv.x, sv.y) + add1(sv.z, sv.w)) >> 23; ex = ex < 1 ? 1 : ex > 251 ? 251 : ex;
    ysc = __uint_as_float((252u - ex) << 23); yinv = __uint_as_float((ex + 2u) << 23);
  }
  if (c16 < 3) {
    const f32x4 k = ((lds_ld<f32x4>(rpa) + lds_ld<f32x4>(rpa + 4352)) + (lds_ld<f32x4>(rpa + 2 * 4352) + lds_ld<f32x4>(rpa + 3 * 4352))) * ysc;
    const u32 k01 = pk_bf(lo2(k)), k23 = pk_bf(hi2(k));
    lds_st<u32>(ra0, k01); lds_st<u32>(ra1, k01 >> 16); lds_st<u32>(ra2, k23); lds_st<u32>(ra3, k23 >> 16);
  }
  fence();
  DMA_ADD("sc1 ", hs5, rWS, vH, lw, LA + 40960, 0);
  fence();
  bar();
  fence();
  STAMP(33);
  {
    const u32x4 R = lds_ld<u32x4>(rra);
    const u32x2 N = lds_ld<u32x2>(rra + 16);
  fence();
  DMA_ADD("sc1 ", hs6, rWS, vH, lw, LA + 49152, 0);
  fence();
  POLL_ISSUE(WS_FU + j * 256 + (lane >> 1) * 8);
  fence();
    const u32 D0 = __builtin_amdgcn_perm(0, R.x, e0), D1 = __builtin_amdgcn_perm(R.y, R.x, e1);
    const u32 D2 = __builtin_amdgcn_perm(N.x, r0 ? R.z : r1 ? R.y : R.x, e2), D3 = __builtin_amdgcn_perm(N.x, r0 ? R.w : R.y, e3);
    const u32 D4 = __builtin_amdgcn_perm(0, N.x, e4), D5 = __builtin_amdgcn_perm(N.y, N.x, e5);
    const u32 E0 = __builtin_amdgcn_alignbit(D1, D0, 16), E1 = __builtin_amdgcn_alignbit(D2, D1, 16), E2 = __builtin_amdgcn_alignbit(D3, D2, 16),
              E3 = __builtin_amdgcn_alignbit(D4, D3, 16), E4 = __builtin_amdgcn_alignbit(D5, D4, 16);
    lds_st<u32x4>(cp + 32, (u32x4){E0, E1, E2, E3});
    lds_st<u32x4>(cp + 32 + 4160, (u32x4){D1, D2, D3, D4});
    lds_st<u32x4>(cp + 32 + 2 * 4160, (u32x4){E1, E2, E3, E4});
    lds_st<u32x4>(cp + 32 + 3 * 4160, (u32x4){D2, D3, D4, D5});
    if (c16 == 0 && lane < 8) {
      const u32 d0 = __builtin_amdgcn_readfirstlane(D0), d1 = __builtin_amdgcn_readfirstlane(D1), f0 = __builtin_amdgcn_readfirstlane(E0);
      const int q = lane >> 1;
      const u32 z2_ = q == 2 ? d0 << 16 : q == 3 ? d0 : 0, z3_ = q == 0 ? d0 << 16 : q == 1 ? d0 : q == 2 ? f0 : d1;
      lds_st<u32x4>(LC_SCR + cl * 16640 + q * 4160 + (lane & 1) * 16, (u32x4){0, 0, (lane & 1) ? z2_ : 0, (lane & 1) ? z3_ : 0});
    }
  }
  MK("WR 0");
  fence();
  DMA_MUL("", su, rW3, v3, stg, 0, cj0, 229376);
  fence();
  MK("WR 0");
  fence();
  DMA_ADD("", su, rW3, v3, stg, 1024, 14336);
  fence();
  STAMP(6);
  u32x2 x1[2];
  f32x4 acc2[2] = {};
  u32x4 hb[4][2];
  const u32 aP = opq(aX + ta * 256);
  WAITV(2);
  fence();
  POLL_CHECK(2, WS_FU + j * 256 + (lane >> 1) * 8, true);
  fence();
  bar();
  fence();
  __builtin_amdgcn_s_setprio(2);
  STAMP(21);
  SKEW();
  hb[0][0] = lds_ld<u32x4>(aP + 0); hb[0][1] = lds_ld<u32x4>(aP + 512);
  hb[1][0] = lds_ld<u32x4>(aP + 4096); hb[1][1] = lds_ld<u32x4>(aP + 4608);
  hb[2][0] = lds_ld<u32x4>(aP + 8192); hb[2][1] = lds_ld<u32x4>(aP + 8704);
  fence();
  hb[3][0] = lds_ld<u32x4>(aP + 12288); hb[3][1] = lds_ld<u32x4>(aP + 12800);
  acc2[0] = mfma_bf(W2f[0], hb[0][0], acc2[0]); acc2[1] = mfma_bf(W2f[0], hb[0][1], acc2[1]); PIN2(acc2[0], acc2[1]); sb();
  so = WS_U + ch * 4096 + c16 * 1024 - 1024;
  fence();
  DMA_ADD("sc1 ", so, rWS, l16, lw + cl * 512, LC_REC + 512, 1024);
  fence();
  hb[0][0] = lds_ld<u32x4>(aP + 16384); hb[0][1] = lds_ld<u32x4>(aP + 16896);
  acc2[0] = mfma_bf(W2f[1], hb[1][0], acc2[0]); acc2[1] = mfma_bf(W2f[1], hb[1][1], acc2[1]); PIN2(acc2[0], acc2[1]); sb();
  hb[1][0] = lds_ld<u32x4>(aP + 20480); hb[1][1] = lds_ld<u32x4>(aP + 20992);
  acc2[0] = mfma_bf(W2f[2], hb[2][0], acc2[0]); acc2[1] = mfma_bf(W2f[2], hb[2][1], acc2[1]); PIN2(acc2[0], acc2[1]); sb();
  MK("WR 1");
  fence();
  DMA_ADD("", su, rW3, v3, stg, 2048, 114688 - 14336);
  fence();
  hb[2][0] = lds_ld<u32x4>(aP + 24576); hb[2][1] = lds_ld<u32x4>(aP + 25088);
  acc2[0] = mfma_bf(W2f[3], hb[3][0], acc2[0]); acc2[1] = mfma_bf(W2f[3], hb[3][1], acc2[1]); PIN2(acc2[0], acc2[1]); sb();
  hb[3][0] = lds_ld<u32x4>(aP + 28672); hb[3][1] = lds_ld<u32x4>(aP + 29184);
  acc2[0] = mfma_bf(W2f[4], hb[0][0], acc2[0]); acc2[1] = mfma_bf(W2f[4], hb[0][1], acc2[1]); PIN2(acc2[0], acc2[1]); sb();
  MK("WR 1");
  fence();
  DMA_ADD("", su, rW3, v3, stg, 3072, 14336);
  fence();
  hb[0][0] = lds_ld<u32x4>(aP + 32768); hb[0][1] = lds_ld<u32x4>(aP + 33280);
  acc2[0] = mfma_bf(W2f[5], hb[1][0], acc2[0]); acc2[1] = mfma_bf(W2f[5], hb[1][1], acc2[1]); PIN2(acc2[0], acc2[1]); sb();
  hb[1][0] = lds_ld<u32x4>(aP + 36864); hb[1][1] = lds_ld<u32x4>(aP + 37376);
  acc2[0] = mfma_bf(W2f[6], hb[2][0], acc2[0]); acc2[1] = mfma_bf(W2f[6], hb[2][1], acc2[1]); PIN2(acc2[0], acc2[1]); sb();
  fence();
  WAITV(3);
  MK("RD_BEGIN 0");
  fence();
  W3f[0][0] = lds_tr2(tb + 0, tb + 1024); W3f[0][1] = lds_tr2(tbx + 0, tbx + 1024);
  fence();
  MK("RD_END");
  hb[2][0] = lds_ld<u32x4>(aP + 40960); hb[2][1] = lds_ld<u32x4>(aP + 41472);
  acc2[0] = mfma_bf(W2f[7], hb[3][0], acc2[0]); acc2[1] = mfma_bf(W2f[7], hb[3][1], acc2[1]); PIN2(acc2[0], acc2[1]); sb();
  MK("WR 2");
  fence();
  DMA_MUL("", su, rW3, v3, stg, 4096, cj1, 229376);
  fence();
  hb[3][0] = lds_ld<u32x4>(aP + 45056); hb[3][1] = lds_ld<u32x4>(aP + 45568);
  acc2[0] = mfma_bf(W2f[8], hb[0][0], acc2[0]); acc2[1] = mfma_bf(W2f[8], hb[0][1], acc2[1]); PIN2(acc2[0], acc2[1]); sb();
  hb[0][0] = lds_ld<u32x4>(aP + 49152); hb[0][1] = lds_ld<u32x4>(aP + 49664);
  acc2[0] = mfma_bf(W2f[9], hb[1][0], acc2[0]); acc2[1] = mfma_bf(W2f[9], hb[1][1], acc2[1]); PIN2(acc2[0], acc2[1]); sb();
  MK("WR 2");
  fence();
  DMA_ADD("", su, rW3, v3, stg, 5120, 14336);
  fence();
  hb[1][0] = lds_ld<u32x4>(aP + 53248); hb[1][1] = lds_ld<u32x4>(aP + 53760);
  acc2[0] = mfma_bf(W2f[10], hb[2][0], acc2[0]); acc2[1] = mfma_bf(W2f[10], hb[2][1], acc2[1]); PIN2(acc2[0], acc2[1]); sb();
  if (c16 == 0 && lane < 32) lds_st<u32x4>(rra, (u32x4){0, 0, 0, 0});
  fence();
  acc2[0] = mfma_bf(W2f[11], hb[3][0], acc2[0]); acc2[1] = mfma_bf(W2f[11], hb[3][1], acc2[1]); PIN2(acc2[0], acc2[1]); sb();
  acc2[0] = mfma_bf(W2f[12], hb[0][0], acc2[0]); acc2[1] = mfma_bf(W2f[12], hb[0][1], acc2[1]); PIN2(acc2[0], acc2[1]); sb();
  MK("WR 0");
  fence();
  DMA_ADD("", su, rW3, v3, stg, 0, 114688 - 14336);
  fence();
  acc2[0] = mfma_bf(W2f[13], hb[1][0], acc2[0]); acc2[1] = mfma_bf(W2f[13], hb[1][1], acc2[1]); PIN2(acc2[0], acc2[1]); sb();
  STAMP(7);
  {
    float sv[4];
#pragma unroll
    for (int t = 0; t < 2; ++t) {
      const f32x2 a = bf2(xn[t].x) + bf2(pk_bf(lo2(acc2[t]))), b = bf2(xn[t].y) + bf2(pk_bf(hi2(acc2[t])));
      x1[t].x = pk_bf(a); x1[t].y = pk_bf(b);
      const f32x2 s = a + b, q = fma2(a, a, b * b);
      sv[2 * t] = add1(s.x, s.y); sv[2 * t + 1] = add1(q.x, q.y);
    }
    auto q0 = __builtin_amdgcn_permlane16_swap(x1[0].x, x1[1].x, false, false);
    auto q1 = __builtin_amdgcn_permlane16_swap(x1[0].y, x1[1].y, false, false);
    const u32x4 xv = {q0[0], q1[0], q0[1], q1[1]};
    ST128(xv, rWS, vO, sXs);
    const float z = fswap16_sum(fswap32_sum(sv[0], sv[2]), fswap32_sum(sv[1], sv[3]));
    lds_st<float>(LC_STX + c16 * 512 + (tk + 32 * g2) * 8 + g1 * 4, z);
  }
  __builtin_amdgcn_s_setprio(0);
  fence();
  STAMP(38);
  MK("WR 0");
  fence();
  DMA_ADD("", su, rW3, v3, stg, 1024, 14336);
  fence();
  WAITV(5);
  MK("RD_BEGIN 1");
  fence();
  W3f[1][0] = lds_tr2(tb + 2048, tb + 3072); W3f[1][1] = lds_tr2(tbx + 2048, tbx + 3072);
  fence();
  MK("RD_END");
  const u32 vY = opq(vH + (WS_X1 - WS_H + 32));
  const u32 vS = opq((lane & 7) < 7 ? WS_ST + p * 7168 + (tid >> 3) * 16 + (lane & 7) * 1024 : 0x7ffffff0u);
  const u32 tc = opq(tb + (stc - stg)), tcx = opq(tc ^ 32);
  const u32 v4 = opq((32 * w + (lane >> 2)) * (D * 2) + 128 * j + vsw);
  const u32 jj0 = cj0, jj1 = cj1, jj2 = cj2, jj3 = cj3, jj4 = cj4, jj5 = cj5, jj6 = cj6;
  fence();
  WAITV(7);
  fence();
  bar();
  fence();
  OPTW(1, {
    const f32x2 t = (lds_ld<f32x2>(LC_STX + lane * 8) + lds_ld<f32x2>(LC_STX + 512 + lane * 8)) + (lds_ld<f32x2>(LC_STX + 1024 + lane * 8) + lds_ld<f32x2>(LC_STX + 1536 + lane * 8));
    const u32x4 v = ((u32x4){__float_as_uint(t.x), tagv.x, __float_as_uint(t.y), tagv.y});
    ST128(v, rWS, l16, WS_ST + (p * 7 + j) * 1024); });
  fence();
  cs3[0] = mfma_bf(W3f[0][0], ones, cs3[0]); cs3[1] = mfma_bf(W3f[0][1], ones, cs3[1]);
  cs3[0] = mfma_bf(W3f[1][0], ones, cs3[0]); cs3[1] = mfma_bf(W3f[1][1], ones, cs3[1]);
  PIN2(cs3[0], cs3[1]);
  fence();
  STAMP(8);
  SKEW();
  f32x4 y0, y1, y2, y3, y4, y5, y6, y7;
  u32x4 sp0;
#define CV_RD(lo, hi, B, oa, ob) asm volatile("ds_read_b64 %0, %3 offset:%c5\n\tds_read_b64 %1, %3 offset:%c6\n\tds_read_b128 %2, %4 offset:%c7" \
    : "=&v"(lo), "=&v"(hi), "=&v"(B) : "v"(aA), "v"(aB), "i"(oa), "i"(oa + 8), "i"(ob))
#define CV_WAIT(lo, hi, B, n) asm volatile("s_waitcnt lgkmcnt(%c3)" : "+v"(lo), "+v"(hi), "+v"(B) : "i"(n))
#define CV_A(i) ((u32x4){Kl##i.x, Kl##i.y, Kh##i.x, Kh##i.y})
  const f32x4 zero4 = {0, 0, 0, 0};
  u32x2 Kl0, Kh0, Kl1, Kh1, Kl2, Kh2, Kl3, Kh3, Kl4, Kh4, Kl5, Kh5, Kl6, Kh6, Kl7, Kh7, Kl8, Kh8, Kl9, Kh9, Kl10, Kh10, Kl11, Kh11, Kl12, Kh12, Kl13, Kh13, Kl14, Kh14, Kl15, Kh15;
  u32x4 B0, B1, B2, B3, B4, B5, B6, B7, B8, B9, B10, B11, B12, B13, B14, B15;
  CV_RD(Kl0, Kh0, B0, 0, 64);
  CV_RD(Kl1, Kh1, B1, 512, 576);
  CV_RD(Kl2, Kh2, B2, 1024, 1088);
  CV_WAIT(Kl0, Kh0, B0, 6);
  y0 = mfma_bf(CV_A(0), B0, zero4); PIN1(y0); sb();
  MK("WR 5");
  fence();
  DMA_MUL("", su, rW3, v3, stg, -57344, cj2, 229376);
  fence();
  CV_RD(Kl3, Kh3, B3, 1536, 1600);
  CV_WAIT(Kl1, Kh1, B1, 6);
  y1 = mfma_bf(CV_A(0), B1, zero4); PIN1(y1); sb();
  y1 = mfma_bf(CV_A(1), B0, y1); PIN1(y1); sb();
  fence();
  WAITV(4);
  MK("RD_BEGIN 2");
  fence();
  W3f[2][0] = lds_tr2(tb + 4096, tb + 5120); W3f[2][1] = lds_tr2(tbx + 4096, tbx + 5120);
  fence();
  MK("RD_END");
  y2 = mfma_bf(CV_A(1), B1, zero4); PIN1(y2); sb();
  CV_WAIT(Kl2, Kh2, B2, 3);
  y2 = mfma_bf(CV_A(0), B2, y2); PIN1(y2); sb();
  MK("WR 5");
  fence();
  DMA_ADD("", su, rW3, v3, stg, -56320, 14336);
  fence();
  y2 = mfma_bf(CV_A(2), B0, y2); PIN1(y2); sb();
  y3 = mfma_bf(CV_A(1), B2, zero4); PIN1(y3); sb();
  y3 = mfma_bf(CV_A(2), B1, y3); PIN1(y3); sb();
  CV_RD(Kl4, Kh4, B4, 2048, 2112);
  y4 = mfma_bf(CV_A(2), B2, zero4); PIN1(y4); sb();
  CV_WAIT(Kl3, Kh3, B3, 3);
  y3 = mfma_bf(CV_A(0), B3, y3); PIN1(y3); sb();
  fence();
  WAITV(3);
  fence();
  asm volatile("s_barrier" ::: "memory");
  fence();
  SIGNAL(WS_FX + p * 128 + j * 8);
  fence();
  STAMP(44);
  y3 = mfma_bf(CV_A(3), B0, y3); PIN1(y3); sb();
  MK("WR 6");
  fence();
  DMA_ADD("", su, rW3, v3, stg, -55296, 114688 - 14336);
  fence();
  y4 = mfma_bf(CV_A(3), B1, y4); PIN1(y4); sb();
  y4 = mfma_bf(CV_A(1), B3, y4); PIN1(y4); sb();
  y5 = mfma_bf(CV_A(2), B3, zero4); PIN1(y5); sb();
  y5 = mfma_bf(CV_A(3), B2, y5); PIN1(y5); sb();
  MK("WR 6");
  fence();
  DMA_ADD("", su, rW3, v3, stg, -54272, 14336);
  fence();
  WAITV(4);
  MK("RD_BEGIN 0");
  fence();
  W3f[3][0] = lds_tr2(tb + 0, tb + 1024); W3f[3][1] = lds_tr2(tbx + 0, tbx + 1024);
  fence();
  MK("RD_END");
  y6 = mfma_bf(CV_A(3), B3, zero4); PIN1(y6); sb();
  CV_RD(Kl5, Kh5, B5, 2560, 2624);
  CV_WAIT(Kl4, Kh4, B4, 3);
  y4 = mfma_bf(CV_A(4), B0, y4); PIN1(y4); sb();
  y4 = mfma_bf(CV_A(0), B4, y4); PIN1(y4); sb();
  y5 = mfma_bf(CV_A(4), B1, y5); PIN1(y5); sb();
  MK("WR 7");
  fence();
  DMA_MUL("", su, rW3, v3, stg, -53248, cj3, 229376);
  fence();
  y5 = mfma_bf(CV_A(1), B4, y5); PIN1(y5); sb();
  y6 = mfma_bf(CV_A(2), B4, y6); PIN1(y6); sb();
  y6 = mfma_bf(CV_A(4), B2, y6); PIN1(y6); sb();
  CV_RD(Kl6, Kh6, B6, 3072, 3136);
  y7 = mfma_bf(CV_A(3), B4, zero4); PIN1(y7); sb();
  MK("WR 7");
  fence();
  DMA_ADD("", su, rW3, v3, stg, -52224, 14336);
  fence();
  y7 = mfma_bf(CV_A(4), B3, y7); PIN1(y7); sb();
  CV_WAIT(Kl5, Kh5, B5, 3);
  y5 = mfma_bf(CV_A(5), B0, y5); PIN1(y5); sb();
  y5 = mfma_bf(CV_A(0), B5, y5); PIN1(y5); sb();
  CV_RD(Kl7, Kh7, B7, 3584, 3648);
  y6 = mfma_bf(CV_A(5), B1, y6); PIN1(y6); sb();
  MK("WR 1");
  fence();
  DMA_ADD("", su, rW3, v3, stg, 2048, 114688 - 14336);
  fence();
  WAITV(5);
  MK("RD_BEGIN 5");
  fence();
  W3f[4][0] = lds_tr2(tA + 0, tA + 1024); W3f[4][1] = lds_tr2(tAx + 0, tAx + 1024);
  fence();
  MK("RD_END");
  y6 = mfma_bf(CV_A(1), B5, y6); PIN1(y6); sb();
  CV_RD(Kl8, Kh8, B8, 64, 0);
  y7 = mfma_bf(CV_A(2), B5, y7); PIN1(y7); sb();
  CV_RD(Kl9, Kh9, B9, 576, 512);
  y7 = mfma_bf(CV_A(5), B2, y7); PIN1(y7); sb();
  CV_WAIT(Kl6, Kh6, B6, 9);
  y6 = mfma_bf(CV_A(6), B0, y6); PIN1(y6); sb();
  MK("WR 1");
  fence();
  DMA_ADD("", su, rW3, v3, stg, 3072, 14336);
  fence();
  y6 = mfma_bf(CV_A(0), B6, y6); PIN1(y6); sb();
  CV_RD(Kl10, Kh10, B10, 1088, 1024);
  y7 = mfma_bf(CV_A(1), B6, y7); PIN1(y7); sb();
  y7 = mfma_bf(CV_A(6), B1, y7); PIN1(y7); sb();
  CV_WAIT(Kl7, Kh7, B7, 9);
  y7 = mfma_bf(CV_A(0), B7, y7); PIN1(y7); sb();
  fence();
  asm volatile("s_waitcnt lgkmcnt(15)" ::: "memory");
  MK("WR 2");
  fence();
  DMA_MUL("", su, rW3, v3, stg, 4096, cj4, 229376);
  fence();
  WAITV(5);
  MK("RD_BEGIN 6");
  fence();
  W3f[5][0] = lds_tr2(tA + 2048, tA + 3072); W3f[5][1] = lds_tr2(tAx + 2048, tAx + 3072);
  fence();
  MK("RD_END");
  y7 = mfma_bf(CV_A(7), B0, y7); PIN1(y7); sb();
  CV_WAIT(Kl8, Kh8, B8, 6);
  y0 = mfma_bf(CV_A(8), B8, y0); PIN1(y0); sb();
  CV_RD(Kl11, Kh11, B11, 1600, 1536);
  CV_WAIT(Kl9, Kh9, B9, 6);
  y1 = mfma_bf(CV_A(8), B9, y1); PIN1(y1); sb();
  y1 = mfma_bf(CV_A(9), B8, y1); PIN1(y1); sb();
  MK("WR 2");
  fence();
  DMA_ADD("", su, rW3, v3, stg, 5120, 14336);
  fence();
  y2 = mfma_bf(CV_A(9), B9, y2); PIN1(y2); sb();
  CV_WAIT(Kl10, Kh10, B10, 3);
  y2 = mfma_bf(CV_A(8), B10, y2); PIN1(y2); sb();
  y2 = mfma_bf(CV_A(10), B8, y2); PIN1(y2); sb();
  fence();
  WAITV(4);
  MK("RD_BEGIN 7");
  fence();
  W3f[6][0] = lds_tr2(tA + 4096, tA + 5120); W3f[6][1] = lds_tr2(tAx + 4096, tAx + 5120);
  fence();
  MK("RD_END");
  y3 = mfma_bf(CV_A(9), B10, y3); PIN1(y3); sb();
  fence();
  asm volatile("s_waitcnt lgkmcnt(15)" ::: "memory");
  MK("WR 0");
  fence();
  DMA_ADD("", su, rW3, v3, stg, 0, 114688 - 14336);
  fence();
  y3 = mfma_bf(CV_A(10), B9, y3); PIN1(y3); sb();
  CV_RD(Kl12, Kh12, B12, 2112, 2048);
  y4 = mfma_bf(CV_A(10), B10, y4); PIN1(y4); sb();
  MK("WR 0");
  fence();
  DMA_ADD("", su, rW3, v3, stg, 1024, 14336);
  fence();
  CV_WAIT(Kl11, Kh11, B11, 3);
  y3 = mfma_bf(CV_A(8), B11, y3); PIN1(y3); sb();
  fence();
  POLL_ISSUE((((lane & 14) != 14 && (u32)(p - 1 + g) < 32u && lane < 48) ? WS_FX + (p - 1 + g) * 128 + ((lane >> 1) & 7) * 8 : 0x7ffffff0u));
  fence();
  WAITV(4);
  MK("RD_BEGIN 1");
  fence();
  W3f[7][0] = lds_tr2(tb + 2048, tb + 3072); W3f[7][1] = lds_tr2(tbx + 2048, tbx + 3072);
  fence();
  MK("RD_END");
  y3 = mfma_bf(CV_A(11), B8, y3); PIN1(y3); sb();
  y4 = mfma_bf(CV_A(11), B9, y4); PIN1(y4); sb();
  y4 = mfma_bf(CV_A(9), B11, y4); PIN1(y4); sb();
  y5 = mfma_bf(CV_A(10), B11, y5); PIN1(y5); sb();
  y5 = mfma_bf(CV_A(11), B10, y5); PIN1(y5); sb();
  y6 = mfma_bf(CV_A(11), B11, y6); PIN1(y6); sb();
  CV_RD(Kl13, Kh13, B13, 2624, 2560);
  CV_WAIT(Kl12, Kh12, B12, 3);
  y4 = mfma_bf(CV_A(12), B8, y4); PIN1(y4); sb();
  y4 = mfma_bf(CV_A(8), B12, y4); PIN1(y4); sb();
  y5 = mfma_bf(CV_A(12), B9, y5); PIN1(y5); sb();
  y5 = mfma_bf(CV_A(9), B12, y5); PIN1(y5); sb();
  y6 = mfma_bf(CV_A(10), B12, y6); PIN1(y6); sb();
  y6 = mfma_bf(CV_A(12), B10, y6); PIN1(y6); sb();
  fence();
  asm volatile("s_waitcnt lgkmcnt(10)" ::: "memory");
  fence();
  POLL_CHECK(0, WS_FX + (p - 1 + g) * 128 + ((lane >> 1) & 7) * 8, ((lane & 14) != 14 && (u32)(p - 1 + g) < 32u && lane < 48));
  fence();
  asm volatile("s_barrier" ::: "memory");
  fence();
  STAMP(10);
  fence();
  LD128(sp0, rWS, vS);
  fence();
  DMA_MUL("sc1 ", so, rWS, vY, lw, LA + 0, cj0, 8 * HROW);
  fence();
  CV_RD(Kl14, Kh14, B14, 3136, 3072);
  y7 = mfma_bf(CV_A(11), B12, y7); PIN1(y7); sb();
  y7 = mfma_bf(CV_A(12), B11, y7); PIN1(y7); sb();
  fence();
  DMA_MUL("sc1 ", so, rWS, vY, lw, LA + 8192, cj1, 8 * HROW);
  fence();
  CV_WAIT(Kl13, Kh13, B13, 3);
  y5 = mfma_bf(CV_A(13), B8, y5); PIN1(y5); sb();
  y5 = mfma_bf(CV_A(8), B13, y5); PIN1(y5); sb();
  fence();
  DMA_MUL("sc1 ", so, rWS, vY, lw, LA + 16384, cj2, 8 * HROW);
  fence();
  WAITV(6);
  MK("RD_BEGIN 2");
  fence();
  W3f[8][0] = lds_tr2(tb + 4096, tb + 5120); W3f[8][1] = lds_tr2(tbx + 4096, tbx + 5120);
  fence();
  MK("RD_END");
  CV_RD(Kl15, Kh15, B15, 3648, 3584);
  y6 = mfma_bf(CV_A(13), B9, y6); PIN1(y6); sb();
  y6 = mfma_bf(CV_A(9), B13, y6); PIN1(y6); sb();
  y7 = mfma_bf(CV_A(10), B13, y7); PIN1(y7); sb();
  fence();
  DMA_MUL("sc1 ", so, rWS, vY, lw, LA + 24576, cj3, 8 * HROW);
  fence();
  y7 = mfma_bf(CV_A(13), B10, y7); PIN1(y7); sb();
  CV_WAIT(Kl14, Kh14, B14, 3);
  y6 = mfma_bf(CV_A(14), B8, y6); PIN1(y6); sb();
  y6 = mfma_bf(CV_A(8), B14, y6); PIN1(y6); sb();
  fence();
  DMA_MUL("sc1 ", so, rWS, vY, lw, LA + 32768, cj4, 8 * HROW);
  fence();
  WAITV(6);
  MK("RD_BEGIN 0");
  fence();
  W3f[9][0] = lds_tr2(tb + 0, tb + 1024); W3f[9][1] = lds_tr2(tbx + 0, tbx + 1024);
  fence();
  MK("RD_END");
  y7 = mfma_bf(CV_A(9), B14, y7); PIN1(y7); sb();
  y7 = mfma_bf(CV_A(14), B9, y7); PIN1(y7); sb();
  CV_WAIT(Kl15, Kh15, B15, 0);
  y7 = mfma_bf(CV_A(8), B15, y7); PIN1(y7); sb();
  fence();
  DMA_MUL("sc1 ", so, rWS, vY, lw, LA + 40960, cj5, 8 * HROW);
  fence();
  y7 = mfma_bf(CV_A(15), B8, y7); PIN1(y7); sb();
  fence();
  DMA_MUL("sc1 ", so, rWS, vY, lw, LA + 49152, cj6, 8 * HROW);
  fence();
  STAMP(34);
  const u32 au = LC_REC + cl * 4608 + 512 + c16 * 1024 + (2 * n16 + g2) * 16 + (g1 ? 0 : 8);
  const u32x2 u0 = lds_ld<u32x2>(au), u1 = lds_ld<u32x2>(au + 512);
  const u32x4 e0_ = {pk_h(lo2(y0)), pk_h(hi2(y0)), pk_h(lo2(y1)), pk_h(hi2(y1))}, e1_ = {pk_h(lo2(y2)), pk_h(hi2(y2)), pk_h(lo2(y3)), pk_h(hi2(y3))};
  const u32x4 e2_ = {pk_h(lo2(y4)), pk_h(hi2(y4)), pk_h(lo2(y5)), pk_h(hi2(y5))}, e3_ = {pk_h(lo2(y6)), pk_h(hi2(y6)), pk_h(lo2(y7)), pk_h(hi2(y7))};
  const u32 aE = opq(stg + l16);
  fence();
  asm volatile("s_waitcnt lgkmcnt(0)" ::: "memory");
  fence();
  lds_st<u32x4>(aE, e0_); lds_st<u32x4>(aE + 1024, e1_); lds_st<u32x4>(aE + 2048, e2_); lds_st<u32x4>(aE + 3072, e3_);
  cs3[0] = mfma_bf(W3f[2][0], ones, cs3[0]); cs3[1] = mfma_bf(W3f[2][1], ones, cs3[1]);
  cs3[0] = mfma_bf(W3f[3][0], ones, cs3[0]); cs3[1] = mfma_bf(W3f[3][1], ones, cs3[1]);
  cs3[0] = mfma_bf(W3f[4][0], ones, cs3[0]); cs3[1] = mfma_bf(W3f[4][1], ones, cs3[1]);
  cs3[0] = mfma_bf(W3f[5][0], ones, cs3[0]); cs3[1] = mfma_bf(W3f[5][1], ones, cs3[1]);
  cs3[0] = mfma_bf(W3f[6][0], ones, cs3[0]); cs3[1] = mfma_bf(W3f[6][1], ones, cs3[1]);
  cs3[0] = mfma_bf(W3f[7][0], ones, cs3[0]); cs3[1] = mfma_bf(W3f[7][1], ones, cs3[1]);
  cs3[0] = mfma_bf(W3f[8][0], ones, cs3[0]); cs3[1] = mfma_bf(W3f[8][1], ones, cs3[1]);
  cs3[0] = mfma_bf(W3f[9][0], ones, cs3[0]); cs3[1] = mfma_bf(W3f[9][1], ones, cs3[1]);
  PIN2(cs3[0], cs3[1]);
  fence();
  bar();
  fence();
  STAMP(9);
  MK("WR 2");
  fence();
  DMA_MUL("", su, rW3, v3, stg, 4096, cj5, 229376);
  fence();
  const u32 aF = opq(LB + cl * 28672 + c16 * 1024 + l16);
  const u32x4 x0_ = lds_ld<u32x4>(aF), x1_ = lds_ld<u32x4>(aF + 7168), x2_ = lds_ld<u32x4>(aF + 14336), x3_ = lds_ld<u32x4>(aF + 21504);
  fence();
  const float dskip = cl ? bf_hi(dsw) : bf_lo(dsw);
  const f32x2 dk0 = bf2(pk_bf((f32x2){bf_hi(u0.y), bf_lo(u0.y)} * bc(dskip))), dk1 = bf2(pk_bf((f32x2){bf_hi(u0.x), bf_lo(u0.x)} * bc(dskip)));
  const f32x2 dk2 = bf2(pk_bf((f32x2){bf_hi(u1.y), bf_lo(u1.y)} * bc(dskip))), dk3 = bf2(pk_bf((f32x2){bf_hi(u1.x), bf_lo(u1.x)} * bc(dskip)));
#define YS(c) bf2(pk_bf(h2f(hadd(hadd(x0_.c, x1_.c), hadd(x2_.c, x3_.c))) * bc(yinv)))
  u32x2 ssa = {pk_bf(YS(x) + dk0), pk_bf(YS(y) + dk1)}, ssb = {pk_bf(YS(z) + dk2), pk_bf(YS(w) + dk3)};
  PIN2(ssa, ssb);
  MK("WR 2");
  fence();
  DMA_ADD("", su, rW3, v3, stg, 5120, 14336);
  fence();
  MK("WR 3");
  fence();
  DMA_ADD("", su, rW3, v3, stc, 0, 114688 - 14336);
  fence();
  STAMP(11);
  fence();
  WAITV(10);
  asm volatile("" : "+v"(sp0));
  {
    const bool act = (lane & 7) < 7;
#pragma unroll 1
    for (u32 sp = 0; sp < (1u << 16) && !__all(!act || (sp0.y == tagv.x && sp0.w == tagv.y)); ++sp) {
      asm volatile("; OPT_BEGIN");
      LD128(sp0, rWS, vS);
      asm volatile("s_waitcnt vmcnt(0)" : "+v"(sp0)::"memory");
      asm volatile("; OPT_END");
    }
    const float a1 = sum8(__uint_as_float(sp0.x)), a2 = sum8(__uint_as_float(sp0.z));
    const float mu = a1 * (1.f / D), var = fmaxf(a2 * (1.f / D) - mu * mu, 0.f);
    const float sd = rbf(__builtin_amdgcn_sqrtf(rbf(rbf(var) + 1e-5f))), rs = __builtin_amdgcn_rcpf(sd);
    if ((lane & 7) == 0) lds_st<f32x4>(LC_STA + (tid >> 3) * 16, (f32x4){rbf(mu), GC1 * rs, sd, GC2 * rs * rs * rs});
  }
  MK("WR 3");
  fence();
  DMA_ADD("", su, rW3, v3, stc, 1024, 14336);
  fence();
  {
    const u32 vs = (16 * n16 + 4 * g) * 2;
    so = WS_S + ch * 4096 + c16 * 1024;
    ST64(ssa, rWS, vs, so);
    ST64(ssb, rWS, vs + 512, so);
  }
  MK("WR 4");
  fence();
  DMA_MUL("", su, rW3, v3, stc, 2048, cj6, 229376);
  fence();
  WAITV(7);
  fence();
  bar();
  fence();
  STAMP(12);
  SKEW();
  u32x4 Hown[4], bx[3][2];
  f32x4 acc3[2][2], kc[2];
  const u32 vHH = opq(vH + (32 - WS_H));
  const u32x4 rT0 = mk_srd(ws + WS_H, 56 * HROW), rT1 = mk_srd(ws + WS_H1, 56 * HROW), rT2 = mk_srd(ws + WS_X1, 56 * HROW), rT3 = mk_srd(ws + WS_H3, 56 * HROW);
  u32 sH = j * (8 * HROW);
  asm volatile("" : "+s"(sH));
  bx[0][0] = lds_ld<u32x4>(aX); bx[0][1] = lds_ld<u32x4>(aX + 256); bx[1][0] = lds_ld<u32x4>(aX + 4096); bx[1][1] = lds_ld<u32x4>(aX + 4096 + 256);
  bx[2][0] = lds_ld<u32x4>(aX + 8192);
  acc3[0][0] = mfma_bf(W3f[0][0], bx[0][0], zero4);
  acc3[0][1] = mfma_bf(W3f[0][1], bx[0][0], zero4);
  PIN2(acc3[0][0], acc3[0][1]); sb();
  MK("WR 4");
  fence();
  DMA_ADD("", su, rW3, v3, stc, 3072, 14336);
  fence();
  bx[2][1] = lds_ld<u32x4>(aX + 8448);
  acc3[1][0] = mfma_bf(W3f[0][0], bx[0][1], zero4);
  acc3[1][1] = mfma_bf(W3f[0][1], bx[0][1], zero4);
  PIN2(acc3[1][0], acc3[1][1]); sb();
  bx[0][0] = lds_ld<u32x4>(aX + 12288);
  acc3[0][0] = mfma_bf(W3f[1][0], bx[1][0], acc3[0][0]);
  acc3[0][1] = mfma_bf(W3f[1][1], bx[1][0], acc3[0][1]);
  PIN2(acc3[0][0], acc3[0][1]); sb();
  MK("WR 0");
  fence();
  DMA_ADD("", su, rW3, v3, stg, 0, 114688 - 14336);
  fence();
  bx[0][1] = lds_ld<u32x4>(aX + 12544);
  acc3[1][0] = mfma_bf(W3f[1][0], bx[1][1], acc3[1][0]);
  acc3[1][1] = mfma_bf(W3f[1][1], bx[1][1], acc3[1][1]);
  PIN2(acc3[1][0], acc3[1][1]); sb();
  fence();
  WAITV(7);
  MK("RD_BEGIN 2");
  fence();
  W3f[10][0] = lds_tr2(tb + 4096, tb + 5120); W3f[10][1] = lds_tr2(tbx + 4096, tbx + 5120);
  fence();
  MK("RD_END");
  bx[1][0] = lds_ld<u32x4>(aX + 16384);
  acc3[0][0] = mfma_bf(W3f[2][0], bx[2][0], acc3[0][0]);
  acc3[0][1] = mfma_bf(W3f[2][1], bx[2][0], acc3[0][1]);
  PIN2(acc3[0][0], acc3[0][1]); sb();
  bx[1][1] = lds_ld<u32x4>(aX + 16640);
  acc3[1][0] = mfma_bf(W3f[2][0], bx[2][1], acc3[1][0]);
  acc3[1][1] = mfma_bf(W3f[2][1], bx[2][1], acc3[1][1]);
  PIN2(acc3[1][0], acc3[1][1]); sb();
  MK("WR 0");
  fence();
  DMA_ADD("", su, rW3, v3, stg, 1024, 14336);
  fence();
  bx[2][0] = lds_ld<u32x4>(aX + 20480);
  acc3[0][0] = mfma_bf(W3f[3][0], bx[0][0], acc3[0][0]);
  acc3[0][1] = mfma_bf(W3f[3][1], bx[0][0], acc3[0][1]);
  PIN2(acc3[0][0], acc3[0][1]); sb();
  bx[2][1] = lds_ld<u32x4>(aX + 20736);
  acc3[1][0] = mfma_bf(W3f[3][0], bx[0][1], acc3[1][0]);
  acc3[1][1] = mfma_bf(W3f[3][1], bx[0][1], acc3[1][1]);
  PIN2(acc3[1][0], acc3[1][1]); sb();
  MK("WR 1");
  fence();
  DMA_MUL("", su, rW4, v4, stg, 2048, jj0, 229376);
  fence();
  WAITV(7);
  MK("RD_BEGIN 3");
  fence();
  W3f[11][0] = lds_tr2(tc + 0, tc + 1024); W3f[11][1] = lds_tr2(tcx + 0, tcx + 1024);
  fence();
  MK("RD_END");
  bx[0][0] = lds_ld<u32x4>(aX + 24576);
  acc3[0][0] = mfma_bf(W3f[4][0], bx[1][0], acc3[0][0]);
  acc3[0][1] = mfma_bf(W3f[4][1], bx[1][0], acc3[0][1]);
  PIN2(acc3[0][0], acc3[0][1]); sb();
  bx[0][1] = lds_ld<u32x4>(aX + 24832);
  acc3[1][0] = mfma_bf(W3f[4][0], bx[1][1], acc3[1][0]);
  acc3[1][1] = mfma_bf(W3f[4][1], bx[1][1], acc3[1][1]);
  PIN2(acc3[1][0], acc3[1][1]); sb();
  bx[1][0] = lds_ld<u32x4>(aX + 28672);
  acc3[0][0] = mfma_bf(W3f[5][0], bx[2][0], acc3[0][0]);
  acc3[0][1] = mfma_bf(W3f[5][1], bx[2][0], acc3[0][1]);
  PIN2(acc3[0][0], acc3[0][1]); sb();
  fence();
  WAITV(5);
  fence();
  asm volatile("s_barrier" ::: "memory");
  fence();
  SIGNAL(WS_FS + j * 256 + p * 8);
  fence();
  MK("WR 1");
  fence();
  DMA_ADD("", su, rW4, v4, stg, 3072, 14336);
  fence();
  bx[1][1] = lds_ld<u32x4>(aX + 28928);
  acc3[1][0] = mfma_bf(W3f[5][0], bx[2][1], acc3[1][0]);
  acc3[1][1] = mfma_bf(W3f[5][1], bx[2][1], acc3[1][1]);
  PIN2(acc3[1][0], acc3[1][1]); sb();
  fence();
  WAITV(4);
  MK("RD_BEGIN 4");
  fence();
  W3f[12][0] = lds_tr2(tc + 2048, tc + 3072); W3f[12][1] = lds_tr2(tcx + 2048, tcx + 3072);
  fence();
  MK("RD_END");
  bx[2][0] = lds_ld<u32x4>(aX + 32768);
  acc3[0][0] = mfma_bf(W3f[6][0], bx[0][0], acc3[0][0]);
  acc3[0][1] = mfma_bf(W3f[6][1], bx[0][0], acc3[0][1]);
  PIN2(acc3[0][0], acc3[0][1]); sb();
  MK("WR 2");
  fence();
  DMA_ADD("", su, rW4, v4, stg, 4096, 64 - 14336);
  fence();
  bx[2][1] = lds_ld<u32x4>(aX + 33024);
  acc3[1][0] = mfma_bf(W3f[6][0], bx[0][1], acc3[1][0]);
  acc3[1][1] = mfma_bf(W3f[6][1], bx[0][1], acc3[1][1]);
  PIN2(acc3[1][0], acc3[1][1]); sb();
  bx[0][0] = lds_ld<u32x4>(aX + 36864);
  acc3[0][0] = mfma_bf(W3f[7][0], bx[1][0], acc3[0][0]);
  acc3[0][1] = mfma_bf(W3f[7][1], bx[1][0], acc3[0][1]);
  PIN2(acc3[0][0], acc3[0][1]); sb();
  bx[0][1] = lds_ld<u32x4>(aX + 37120);
  acc3[1][0] = mfma_bf(W3f[7][0], bx[1][1], acc3[1][0]);
  acc3[1][1] = mfma_bf(W3f[7][1], bx[1][1], acc3[1][1]);
  PIN2(acc3[1][0], acc3[1][1]); sb();
  MK("WR 2");
  fence();
  DMA_ADD("", su, rW4, v4, stg, 5120, 14336);
  fence();
  WAITV(4);
  MK("RD_BEGIN 0");
  fence();
  W3f[13][0] = lds_tr2(tb + 0, tb + 1024); W3f[13][1] = lds_tr2(tbx + 0, tbx + 1024);
  fence();
  MK("RD_END");
  bx[1][0] = lds_ld<u32x4>(aX + 40960);
  acc3[0][0] = mfma_bf(W3f[8][0], bx[2][0], acc3[0][0]);
  acc3[0][1] = mfma_bf(W3f[8][1], bx[2][0], acc3[0][1]);
  PIN2(acc3[0][0], acc3[0][1]); sb();
  bx[1][1] = lds_ld<u32x4>(aX + 41216);
  acc3[1][0] = mfma_bf(W3f[8][0], bx[2][1], acc3[1][0]);
  acc3[1][1] = mfma_bf(W3f[8][1], bx[2][1], acc3[1][1]);
  PIN2(acc3[1][0], acc3[1][1]); sb();
  MK("WR 3");
  fence();
  DMA_MUL("", su, rW4, v4, stc, 0, jj1, 229376);
  fence();
  bx[2][0] = lds_ld<u32x4>(aX + 45056);
  acc3[0][0] = mfma_bf(W3f[9][0], bx[0][0], acc3[0][0]);
  acc3[0][1] = mfma_bf(W3f[9][1], bx[0][0], acc3[0][1]);
  PIN2(acc3[0][0], acc3[0][1]); sb();
  bx[2][1] = lds_ld<u32x4>(aX + 45312);
  acc3[1][0] = mfma_bf(W3f[9][0], bx[0][1], acc3[1][0]);
  acc3[1][1] = mfma_bf(W3f[9][1], bx[0][1], acc3[1][1]);
  PIN2(acc3[1][0], acc3[1][1]); sb();
  fence();
  WAITV(3);
  MK("RD_BEGIN 1");
  fence();
  W4f[0][0] = lds_tr2(tb + 2048, tb + 3072); W4f[0][1] = lds_tr2(tbx + 2048, tbx + 3072);
  fence();
  MK("RD_END");
  bx[0][0] = lds_ld<u32x4>(aX + 49152);
  acc3[0][0] = mfma_bf(W3f[10][0], bx[1][0], acc3[0][0]);
  acc3[0][1] = mfma_bf(W3f[10][1], bx[1][0], acc3[0][1]);
  PIN2(acc3[0][0], acc3[0][1]); sb();
  MK("WR 3");
  fence();
  DMA_ADD("", su, rW4, v4, stc, 1024, 14336);
  fence();
  cs3[0] = mfma_bf(W3f[10][0], ones, cs3[0]); cs3[1] = mfma_bf(W3f[10][1], ones, cs3[1]);
  cs3[0] = mfma_bf(W3f[11][0], ones, cs3[0]); cs3[1] = mfma_bf(W3f[11][1], ones, cs3[1]);
  cs3[0] = mfma_bf(W3f[12][0], ones, cs3[0]); cs3[1] = mfma_bf(W3f[12][1], ones, cs3[1]);
  cs3[0] = mfma_bf(W3f[13][0], ones, cs3[0]); cs3[1] = mfma_bf(W3f[13][1], ones, cs3[1]);
  PIN2(cs3[0], cs3[1]);
  fence();
  bx[0][1] = lds_ld<u32x4>(aX + 49408);
  acc3[1][0] = mfma_bf(W3f[10][0], bx[1][1], acc3[1][0]);
  acc3[1][1] = mfma_bf(W3f[10][1], bx[1][1], acc3[1][1]);
  PIN2(acc3[1][0], acc3[1][1]); sb();
  bx[1][0] = lds_ld<u32x4>(aX + 53248);
  acc3[0][0] = mfma_bf(W3f[11][0], bx[2][0], acc3[0][0]);
  acc3[0][1] = mfma_bf(W3f[11][1], bx[2][0], acc3[0][1]);
  PIN2(acc3[0][0], acc3[0][1]); sb();
  MK("WR 4");
  fence();
  DMA_ADD("", su, rW4, v4, stc, 2048, 64 - 14336);
  fence();
  bx[1][1] = lds_ld<u32x4>(aX + 53504);
  acc3[1][0] = mfma_bf(W3f[11][0], bx[2][1], acc3[1][0]);
  acc3[1][1] = mfma_bf(W3f[11][1], bx[2][1], acc3[1][1]);
  PIN2(acc3[1][0], acc3[1][1]); sb();
  fence();
  WAITV(3);
  MK("RD_BEGIN 2");
  fence();
  W4f[0][2] = lds_tr2(tb + 4096, tb + 5120); W4f[0][3] = lds_tr2(tbx + 4096, tbx + 5120);
  fence();
  MK("RD_END");
  bx[2][0] = lds_ld<u32x4>(aX + 512);
  kc[0] = lds_ld<f32x4>(aSt + 0);
  acc3[0][0] = mfma_bf(W3f[12][0], bx[0][0], acc3[0][0]);
  acc3[0][1] = mfma_bf(W3f[12][1], bx[0][0], acc3[0][1]);
  PIN2(acc3[0][0], acc3[0][1]); sb();
  MK("WR 4");
  fence();
  DMA_ADD("", su, rW4, v4, stc, 3072, 14336);
  fence();
  bx[2][1] = lds_ld<u32x4>(aX + 768);
  kc[1] = lds_ld<f32x4>(aSt + 256);
  acc3[1][0] = mfma_bf(W3f[12][0], bx[0][1], acc3[1][0]);
  acc3[1][1] = mfma_bf(W3f[12][1], bx[0][1], acc3[1][1]);
  PIN2(acc3[1][0], acc3[1][1]); sb();
  MK("WR 0");
  fence();
  DMA_MUL("", su, rW4, v4, stg, 0, jj2, 229376);
  fence();
  bx[0][0] = lds_ld<u32x4>(aX + 4608);
  acc3[0][0] = mfma_bf(W3f[13][0], bx[1][0], acc3[0][0]);
  acc3[0][1] = mfma_bf(W3f[13][1], bx[1][0], acc3[0][1]);
  PIN2(acc3[0][0], acc3[0][1]); sb();
  MK("WR 0");
  fence();
  DMA_ADD("", su, rW4, v4, stg, 1024, 14336);
  fence();
  bx[0][1] = lds_ld<u32x4>(aX + 4864);
  acc3[1][0] = mfma_bf(W3f[13][0], bx[1][1], acc3[1][0]);
  acc3[1][1] = mfma_bf(W3f[13][1], bx[1][1], acc3[1][1]);
  PIN2(acc3[1][0], acc3[1][1]); sb();
  MK("WR 1");
  fence();
  DMA_ADD("", su, rW4, v4, stg, 2048, 64 - 14336);
  fence();
  STAMP(22);
  MK("WR 1");
  fence();
  DMA_ADD("", su, rW4, v4, stg, 3072, 14336);
  fence();
  WAITV(6);
  MK("RD_BEGIN 3");
  fence();
  W4f[1][0] = lds_tr2(tc + 0, tc + 1024); W4f[1][1] = lds_tr2(tcx + 0, tcx + 1024);
  fence();
  MK("RD_END");
  fence();
  Hown[0] = (u32x4){pk_bf(gelu2(lo2(acc3[0][0]), lo2(cs3[0]), kc[0])), pk_bf(gelu2(hi2(acc3[0][0]), hi2(cs3[0]), kc[0])), pk_bf(gelu2(lo2(acc3[0][1]), lo2(cs3[1]), kc[0])), pk_bf(gelu2(hi2(acc3[0][1]), hi2(cs3[1]), kc[0]))}; PIN1(Hown[0]);
  fence();
  asm volatile("s_waitcnt lgkmcnt(0)" ::: "memory");
  fence();
  ST128(Hown[0], rT0, vHH, sH);
  fence();
  MK("WR 2");
  fence();
  DMA_MUL("", su, rW4, v4, stg, 4096, jj3, 229376);
  fence();
  WAITV(6);
  MK("RD_BEGIN 4");
  fence();
  W4f[1][2] = lds_tr2(tc + 2048, tc + 3072); W4f[1][3] = lds_tr2(tcx + 2048, tcx + 3072);
  fence();
  MK("RD_END");
  MK("WR 2");
  fence();
  DMA_ADD("", su, rW4, v4, stg, 5120, 14336);
  fence();
  WAITV(5);
  MK("RD_BEGIN 0");
  fence();
  W4f[2][0] = lds_tr2(tb + 0, tb + 1024); W4f[2][1] = lds_tr2(tbx + 0, tbx + 1024);
  fence();
  MK("RD_END");
  MK("WR 3");
  fence();
  DMA_ADD("", su, rW4, v4, stc, 0, 64 - 14336);
  fence();
  Hown[1] = (u32x4){pk_bf(gelu2(lo2(acc3[1][0]), lo2(cs3[0]), kc[1])), pk_bf(gelu2(hi2(acc3[1][0]), hi2(cs3[0]), kc[1])), pk_bf(gelu2(lo2(acc3[1][1]), lo2(cs3[1]), kc[1])), pk_bf(gelu2(hi2(acc3[1][1]), hi2(cs3[1]), kc[1]))}; PIN1(Hown[1]);
  fence();
  asm volatile("s_waitcnt lgkmcnt(0)" ::: "memory");
  fence();
  MK("WR 3");
  fence();
  DMA_ADD("", su, rW4, v4, stc, 1024, 14336);
  fence();
  STAMP(13);
  bx[1][0] = lds_ld<u32x4>(aX + 8704);
  acc3[0][0] = mfma_bf(W3f[0][0], bx[2][0], zero4);
  acc3[0][1] = mfma_bf(W3f[0][1], bx[2][0], zero4);
  PIN2(acc3[0][0], acc3[0][1]); sb();
  fence();
  ST128(Hown[1], rT1, vHH, sH);
  fence();
  WAITV(6);
  MK("RD_BEGIN 1");
  fence();
  W4f[2][2] = lds_tr2(tb + 2048, tb + 3072); W4f[2][3] = lds_tr2(tbx + 2048, tbx + 3072);
  fence();
  MK("RD_END");
  bx[1][1] = lds_ld<u32x4>(aX + 8960);
  acc3[1][0] = mfma_bf(W3f[0][0], bx[2][1], zero4);
  acc3[1][1] = mfma_bf(W3f[0][1], bx[2][1], zero4);
  PIN2(acc3[1][0], acc3[1][1]); sb();
  MK("WR 4");
  fence();
  DMA_MUL("", su, rW4, v4, stc, 2048, jj4, 229376);
  fence();
  bx[2][0] = lds_ld<u32x4>(aX + 12800);
  acc3[0][0] = mfma_bf(W3f[1][0], bx[0][0], acc3[0][0]);
  acc3[0][1] = mfma_bf(W3f[1][1], bx[0][0], acc3[0][1]);
  PIN2(acc3[0][0], acc3[0][1]); sb();
  bx[2][1] = lds_ld<u32x4>(aX + 13056);
  acc3[1][0] = mfma_bf(W3f[1][0], bx[0][1], acc3[1][0]);
  acc3[1][1] = mfma_bf(W3f[1][1], bx[0][1], acc3[1][1]);
  PIN2(acc3[1][0], acc3[1][1]); sb();
  MK("WR 4");
  fence();
  DMA_ADD("", su, rW4, v4, stc, 3072, 14336);
  fence();
  WAITV(5);
  MK("RD_BEGIN 2");
  fence();
  W4f[3][0] = lds_tr2(tb + 4096, tb + 5120); W4f[3][1] = lds_tr2(tbx + 4096, tbx + 5120);
  fence();
  MK("RD_END");
  bx[0][0] = lds_ld<u32x4>(aX + 16896);
  acc3[0][0] = mfma_bf(W3f[2][0], bx[1][0], acc3[0][0]);
  acc3[0][1] = mfma_bf(W3f[2][1], bx[1][0], acc3[0][1]);
  PIN2(acc3[0][0], acc3[0][1]); sb();
  bx[0][1] = lds_ld<u32x4>(aX + 17152);
  acc3[1][0] = mfma_bf(W3f[2][0], bx[1][1], acc3[1][0]);
  acc3[1][1] = mfma_bf(W3f[2][1], bx[1][1], acc3[1][1]);
  PIN2(acc3[1][0], acc3[1][1]); sb();
  MK("WR 0");
  fence();
  DMA_ADD("", su, rW4, v4, stg, 0, 64 - 14336);
  fence();
  bx[1][0] = lds_ld<u32x4>(aX + 20992);
  acc3[0][0] = mfma_bf(W3f[3][0], bx[2][0], acc3[0][0]);
  acc3[0][1] = mfma_bf(W3f[3][1], bx[2][0], acc3[0][1]);
  PIN2(acc3[0][0], acc3[0][1]); sb();
  fence();
  WAITV(4);
  MK("RD_BEGIN 3");
  fence();
  W4f[3][2] = lds_tr2(tc + 0, tc + 1024); W4f[3][3] = lds_tr2(tcx + 0, tcx + 1024);
  fence();
  MK("RD_END");
  bx[1][1] = lds_ld<u32x4>(aX + 21248);
  acc3[1][0] = mfma_bf(W3f[3][0], bx[2][1], acc3[1][0]);
  acc3[1][1] = mfma_bf(W3f[3][1], bx[2][1], acc3[1][1]);
  PIN2(acc3[1][0], acc3[1][1]); sb();
  MK("WR 0");
  fence();
  DMA_ADD("", su, rW4, v4, stg, 1024, 14336);
  fence();
  bx[2][0] = lds_ld<u32x4>(aX + 25088);
  acc3[0][0] = mfma_bf(W3f[4][0], bx[0][0], acc3[0][0]);
  acc3[0][1] = mfma_bf(W3f[4][1], bx[0][0], acc3[0][1]);
  PIN2(acc3[0][0], acc3[0][1]); sb();
  bx[2][1] = lds_ld<u32x4>(aX + 25344);
  acc3[1][0] = mfma_bf(W3f[4][0], bx[0][1], acc3[1][0]);
  acc3[1][1] = mfma_bf(W3f[4][1], bx[0][1], acc3[1][1]);
  PIN2(acc3[1][0], acc3[1][1]); sb();
  MK("WR 1");
  fence();
  DMA_MUL("", su, rW4, v4, stg, 2048, jj5, 229376);
  fence();
  bx[0][0] = lds_ld<u32x4>(aX + 29184);
  acc3[0][0] = mfma_bf(W3f[5][0], bx[1][0], acc3[0][0]);
  acc3[0][1] = mfma_bf(W3f[5][1], bx[1][0], acc3[0][1]);
  PIN2(acc3[0][0], acc3[0][1]); sb();
  bx[0][1] = lds_ld<u32x4>(aX + 29440);
  acc3[1][0] = mfma_bf(W3f[5][0], bx[1][1], acc3[1][0]);
  acc3[1][1] = mfma_bf(W3f[5][1], bx[1][1], acc3[1][1]);
  PIN2(acc3[1][0], acc3[1][1]); sb();
  MK("WR 1");
  fence();
  DMA_ADD("", su, rW4, v4, stg, 3072, 14336);
  fence();
  WAITV(4);
  MK("RD_BEGIN 4");
  fence();
  W4f[4][0] = lds_tr2(tc + 2048, tc + 3072); W4f[4][1] = lds_tr2(tcx + 2048, tcx + 3072);
  fence();
  MK("RD_END");
  bx[1][0] = lds_ld<u32x4>(aX + 33280);
  acc3[0][0] = mfma_bf(W3f[6][0], bx[2][0], acc3[0][0]);
  acc3[0][1] = mfma_bf(W3f[6][1], bx[2][0], acc3[0][1]);
  PIN2(acc3[0][0], acc3[0][1]); sb();
  fence();
  WAITV(6);
  fence();
  asm volatile("s_barrier" ::: "memory");
  fence();
  SIGNAL(WS_FA + p * 128 + j * 8);
  fence();
  bx[1][1] = lds_ld<u32x4>(aX + 33536);
  acc3[1][0] = mfma_bf(W3f[6][0], bx[2][1], acc3[1][0]);
  acc3[1][1] = mfma_bf(W3f[6][1], bx[2][1], acc3[1][1]);
  PIN2(acc3[1][0], acc3[1][1]); sb();
  MK("WR 2");
  fence();
  DMA_ADD("", su, rW4, v4, stg, 4096, 64 - 14336);
  fence();
  bx[2][0] = lds_ld<u32x4>(aX + 37376);
  acc3[0][0] = mfma_bf(W3f[7][0], bx[0][0], acc3[0][0]);
  acc3[0][1] = mfma_bf(W3f[7][1], bx[0][0], acc3[0][1]);
  PIN2(acc3[0][0], acc3[0][1]); sb();
  bx[2][1] = lds_ld<u32x4>(aX + 37632);
  acc3[1][0] = mfma_bf(W3f[7][0], bx[0][1], acc3[1][0]);
  acc3[1][1] = mfma_bf(W3f[7][1], bx[0][1], acc3[1][1]);
  PIN2(acc3[1][0], acc3[1][1]); sb();
  MK("WR 2");
  fence();
  DMA_ADD("", su, rW4, v4, stg, 5120, 14336);
  fence();
  WAITV(4);
  MK("RD_BEGIN 0");
  fence();
  W4f[4][2] = lds_tr2(tb + 0, tb + 1024); W4f[4][3] = lds_tr2(tbx + 0, tbx + 1024);
  fence();
  MK("RD_END");
  bx[0][0] = lds_ld<u32x4>(aX + 41472);
  acc3[0][0] = mfma_bf(W3f[8][0], bx[1][0], acc3[0][0]);
  acc3[0][1] = mfma_bf(W3f[8][1], bx[1][0], acc3[0][1]);
  PIN2(acc3[0][0], acc3[0][1]); sb();
  bx[0][1] = lds_ld<u32x4>(aX + 41728);
  acc3[1][0] = mfma_bf(W3f[8][0], bx[1][1], acc3[1][0]);
  acc3[1][1] = mfma_bf(W3f[8][1], bx[1][1], acc3[1][1]);
  PIN2(acc3[1][0], acc3[1][1]); sb();
  MK("WR 3");
  fence();
  DMA_MUL("", su, rW4, v4, stc, 0, jj6, 229376);
  fence();
  bx[1][0] = lds_ld<u32x4>(aX + 45568);
  acc3[0][0] = mfma_bf(W3f[9][0], bx[2][0], acc3[0][0]);
  acc3[0][1] = mfma_bf(W3f[9][1], bx[2][0], acc3[0][1]);
  PIN2(acc3[0][0], acc3[0][1]); sb();
  bx[1][1] = lds_ld<u32x4>(aX + 45824);
  acc3[1][0] = mfma_bf(W3f[9][0], bx[2][1], acc3[1][0]);
  acc3[1][1] = mfma_bf(W3f[9][1], bx[2][1], acc3[1][1]);
  PIN2(acc3[1][0], acc3[1][1]); sb();
  MK("WR 3");
  fence();
  DMA_ADD("", su, rW4, v4, stc, 1024, 14336);
  fence();
  WAITV(4);
  MK("RD_BEGIN 1");
  fence();
  W4f[5][0] = lds_tr2(tb + 2048, tb + 3072); W4f[5][1] = lds_tr2(tbx + 2048, tbx + 3072);
  fence();
  MK("RD_END");
  bx[2][0] = lds_ld<u32x4>(aX + 49664);
  acc3[0][0] = mfma_bf(W3f[10][0], bx[0][0], acc3[0][0]);
  acc3[0][1] = mfma_bf(W3f[10][1], bx[0][0], acc3[0][1]);
  PIN2(acc3[0][0], acc3[0][1]); sb();
  bx[2][1] = lds_ld<u32x4>(aX + 49920);
  acc3[1][0] = mfma_bf(W3f[10][0], bx[0][1], acc3[1][0]);
  acc3[1][1] = mfma_bf(W3f[10][1], bx[0][1], acc3[1][1]);
  PIN2(acc3[1][0], acc3[1][1]); sb();
  MK("WR 4");
  fence();
  DMA_ADD("", su, rW4, v4, stc, 2048, 64 - 14336);
  fence();
  bx[0][0] = lds_ld<u32x4>(aX + 53760);
  acc3[0][0] = mfma_bf(W3f[11][0], bx[1][0], acc3[0][0]);
  acc3[0][1] = mfma_bf(W3f[11][1], bx[1][0], acc3[0][1]);
  PIN2(acc3[0][0], acc3[0][1]); sb();
  MK("WR 4");
  fence();
  DMA_ADD("", su, rW4, v4, stc, 3072, 14336);
  fence();
  bx[0][1] = lds_ld<u32x4>(aX + 54016);
  acc3[1][0] = mfma_bf(W3f[11][0], bx[1][1], acc3[1][0]);
  acc3[1][1] = mfma_bf(W3f[11][1], bx[1][1], acc3[1][1]);
  PIN2(acc3[1][0], acc3[1][1]); sb();
  fence();
  WAITV(4);
  MK("RD_BEGIN 2");
  fence();
  W4f[5][2] = lds_tr2(tb + 4096, tb + 5120); W4f[5][3] = lds_tr2(tbx + 4096, tbx + 5120);
  fence();
  MK("RD_END");
  fence();
  POLL_ISSUE((lane < 14 ? WS_FA + p * 128 + (lane >> 1) * 8 : 0x7ffffff0u));
  fence();
  kc[0] = lds_ld<f32x4>(aSt + 512);
  acc3[0][0] = mfma_bf(W3f[12][0], bx[2][0], acc3[0][0]);
  acc3[0][1] = mfma_bf(W3f[12][1], bx[2][0], acc3[0][1]);
  PIN2(acc3[0][0], acc3[0][1]); sb();
  kc[1] = lds_ld<f32x4>(aSt + 768);
  acc3[1][0] = mfma_bf(W3f[12][0], bx[2][1], acc3[1][0]);
  acc3[1][1] = mfma_bf(W3f[12][1], bx[2][1], acc3[1][1]);
  PIN2(acc3[1][0], acc3[1][1]); sb();
  acc3[0][0] = mfma_bf(W3f[13][0], bx[0][0], acc3[0][0]);
  acc3[0][1] = mfma_bf(W3f[13][1], bx[0][0], acc3[0][1]);
  PIN2(acc3[0][0], acc3[0][1]); sb();
  acc3[1][0] = mfma_bf(W3f[13][0], bx[0][1], acc3[1][0]);
  acc3[1][1] = mfma_bf(W3f[13][1], bx[0][1], acc3[1][1]);
  PIN2(acc3[1][0], acc3[1][1]); sb();
  fence();
  WAITV(2);
  MK("RD_BEGIN 3");
  fence();
  W4f[6][0] = lds_tr2(tc + 0, tc + 1024); W4f[6][1] = lds_tr2(tcx + 0, tcx + 1024);
  fence();
  MK("RD_END");
  STAMP(23);
  fence();
  WAITV(0);
  MK("RD_BEGIN 4");
  fence();
  W4f[6][2] = lds_tr2(tc + 2048, tc + 3072); W4f[6][3] = lds_tr2(tcx + 2048, tcx + 3072);
  fence();
  MK("RD_END");
  u32x4 HA[6][2], HB[6][2];
  f32x4 acc[2][4];
  const u32 rw0 = opq(LR + w * 8192 + l16), rr0 = opq(LR + lw + l16), rw1 = opq(rw0 + 65536), rr1 = opq(rr0 + 65536);
#define HLD(d, r, s) asm volatile("s_mul_i32 %1, %2, %c5\n\tbuffer_load_dwordx4 %0, %3, %4, %1 offen sc1" : "=v"(d), "=&s"(so) : "s"(s), "v"(vHH), "s"(r), "i"(8 * HROW) : "memory")
  fence();
  POLL_CHECK(0, WS_FA + p * 128 + (lane >> 1) * 8, lane < 14);
  fence();
  bar();
  fence();
  STAMP(14);
  fence();
  HLD(HA[0][0], rT0, jj1);
  fence();
  HLD(HA[0][1], rT1, jj1);
  fence();
  HLD(HA[1][0], rT0, jj2);
  fence();
  HLD(HA[1][1], rT1, jj2);
  fence();
  Hown[2] = (u32x4){pk_bf(gelu2(lo2(acc3[0][0]), lo2(cs3[0]), kc[0])), pk_bf(gelu2(hi2(acc3[0][0]), hi2(cs3[0]), kc[0])), pk_bf(gelu2(lo2(acc3[0][1]), lo2(cs3[1]), kc[0])), pk_bf(gelu2(hi2(acc3[0][1]), hi2(cs3[1]), kc[0]))}; PIN1(Hown[2]);
  fence();
  ST128(Hown[2], rT2, vHH, sH);
  fence();
  HLD(HA[2][0], rT0, jj3);
  fence();
  HLD(HA[2][1], rT1, jj3);
  fence();
  Hown[3] = (u32x4){pk_bf(gelu2(lo2(acc3[1][0]), lo2(cs3[0]), kc[1])), pk_bf(gelu2(hi2(acc3[1][0]), hi2(cs3[0]), kc[1])), pk_bf(gelu2(lo2(acc3[1][1]), lo2(cs3[1]), kc[1])), pk_bf(gelu2(hi2(acc3[1][1]), hi2(cs3[1]), kc[1]))}; PIN1(Hown[3]);
  fence();
  ST128(Hown[3], rT3, vHH, sH);
  fence();
  STAMP(15);
  fence();
  HLD(HA[3][0], rT0, jj4);
  fence();
  acc[0][0] = mfma_bf(W4f[0][0], Hown[0], zero4); PIN1(acc[0][0]);
  acc[0][1] = mfma_bf(W4f[0][1], Hown[0], zero4); PIN1(acc[0][1]);
  sb();
  fence();
  HLD(HA[3][1], rT1, jj4);
  fence();
  acc[0][2] = mfma_bf(W4f[0][2], Hown[0], zero4); PIN1(acc[0][2]);
  acc[0][3] = mfma_bf(W4f[0][3], Hown[0], zero4); PIN1(acc[0][3]);
  sb();
  acc[1][0] = mfma_bf(W4f[0][0], Hown[1], zero4); PIN1(acc[1][0]);
  acc[1][1] = mfma_bf(W4f[0][1], Hown[1], zero4); PIN1(acc[1][1]);
  sb();
  fence();
  HLD(HA[4][0], rT0, jj5);
  fence();
  acc[1][2] = mfma_bf(W4f[0][2], Hown[1], zero4); PIN1(acc[1][2]);
  acc[1][3] = mfma_bf(W4f[0][3], Hown[1], zero4); PIN1(acc[1][3]);
  sb();
  fence();
  WAITV(10);
  asm volatile("" : "+v"(HA[0][0]));
  acc[0][0] = mfma_bf(W4f[1][0], HA[0][0], acc[0][0]); PIN1(acc[0][0]);
  acc[0][1] = mfma_bf(W4f[1][1], HA[0][0], acc[0][1]); PIN1(acc[0][1]);
  sb();
  fence();
  HLD(HA[4][1], rT1, jj5);
  fence();
  acc[0][2] = mfma_bf(W4f[1][2], HA[0][0], acc[0][2]); PIN1(acc[0][2]);
  acc[0][3] = mfma_bf(W4f[1][3], HA[0][0], acc[0][3]); PIN1(acc[0][3]);
  sb();
  fence();
  WAITV(10);
  asm volatile("" : "+v"(HA[0][1]));
  acc[1][0] = mfma_bf(W4f[1][0], HA[0][1], acc[1][0]); PIN1(acc[1][0]);
  acc[1][1] = mfma_bf(W4f[1][1], HA[0][1], acc[1][1]); PIN1(acc[1][1]);
  sb();
  fence();
  HLD(HA[5][0], rT0, jj6);
  fence();
  acc[1][2] = mfma_bf(W4f[1][2], HA[0][1], acc[1][2]); PIN1(acc[1][2]);
  acc[1][3] = mfma_bf(W4f[1][3], HA[0][1], acc[1][3]); PIN1(acc[1][3]);
  sb();
  fence();
  WAITV(10);
  asm volatile("" : "+v"(HA[1][0]));
  acc[0][0] = mfma_bf(W4f[2][0], HA[1][0], acc[0][0]); PIN1(acc[0][0]);
  acc[0][1] = mfma_bf(W4f[2][1], HA[1][0], acc[0][1]); PIN1(acc[0][1]);
  sb();
  fence();
  HLD(HA[5][1], rT1, jj6);
  fence();
  acc[0][2] = mfma_bf(W4f[2][2], HA[1][0], acc[0][2]); PIN1(acc[0][2]);
  acc[0][3] = mfma_bf(W4f[2][3], HA[1][0], acc[0][3]); PIN1(acc[0][3]);
  sb();
  fence();
  WAITV(10);
  asm volatile("" : "+v"(HA[1][1]));
  acc[1][0] = mfma_bf(W4f[2][0], HA[1][1], acc[1][0]); PIN1(acc[1][0]);
  acc[1][1] = mfma_bf(W4f[2][1], HA[1][1], acc[1][1]); PIN1(acc[1][1]);
  acc[1][2] = mfma_bf(W4f[2][2], HA[1][1], acc[1][2]); PIN1(acc[1][2]);
  acc[1][3] = mfma_bf(W4f[2][3], HA[1][1], acc[1][3]); PIN1(acc[1][3]);
  sb();
  fence();
  WAITV(6);
  fence();
  asm volatile("s_barrier" ::: "memory");
  fence();
  SIGNAL(WS_FB + p * 128 + j * 8);
  fence();
  WAITV(8);
  asm volatile("" : "+v"(HA[2][0]));
  acc[0][0] = mfma_bf(W4f[3][0], HA[2][0], acc[0][0]); PIN1(acc[0][0]);
  acc[0][1] = mfma_bf(W4f[3][1], HA[2][0], acc[0][1]); PIN1(acc[0][1]);
  acc[0][2] = mfma_bf(W4f[3][2], HA[2][0], acc[0][2]); PIN1(acc[0][2]);
  acc[0][3] = mfma_bf(W4f[3][3], HA[2][0], acc[0][3]); PIN1(acc[0][3]);
  sb();
  fence();
  POLL_ISSUE(WS_FS + j * 256 + (lane >> 1) * 8);
  fence();
  WAITV(7);
  asm volatile("" : "+v"(HA[2][1]));
  acc[1][0] = mfma_bf(W4f[3][0], HA[2][1], acc[1][0]); PIN1(acc[1][0]);
  acc[1][1] = mfma_bf(W4f[3][1], HA[2][1], acc[1][1]); PIN1(acc[1][1]);
  acc[1][2] = mfma_bf(W4f[3][2], HA[2][1], acc[1][2]); PIN1(acc[1][2]);
  acc[1][3] = mfma_bf(W4f[3][3], HA[2][1], acc[1][3]); PIN1(acc[1][3]);
  sb();
  fence();
  WAITV(5);
  asm volatile("" : "+v"(HA[3][0]));
  acc[0][0] = mfma_bf(W4f[4][0], HA[3][0], acc[0][0]); PIN1(acc[0][0]);
  acc[0][1] = mfma_bf(W4f[4][1], HA[3][0], acc[0][1]); PIN1(acc[0][1]);
  acc[0][2] = mfma_bf(W4f[4][2], HA[3][0], acc[0][2]); PIN1(acc[0][2]);
  acc[0][3] = mfma_bf(W4f[4][3], HA[3][0], acc[0][3]); PIN1(acc[0][3]);
  sb();
  fence();
  WAITV(4);
  asm volatile("" : "+v"(HA[3][1]));
  acc[1][0] = mfma_bf(W4f[4][0], HA[3][1], acc[1][0]); PIN1(acc[1][0]);
  acc[1][1] = mfma_bf(W4f[4][1], HA[3][1], acc[1][1]); PIN1(acc[1][1]);
  acc[1][2] = mfma_bf(W4f[4][2], HA[3][1], acc[1][2]); PIN1(acc[1][2]);
  acc[1][3] = mfma_bf(W4f[4][3], HA[3][1], acc[1][3]); PIN1(acc[1][3]);
  sb();
  fence();
  POLL_CHECK(0, WS_FS + j * 256 + (lane >> 1) * 8, true);
  fence();
  POLL_ISSUE((lane < 14 ? WS_FB + p * 128 + (lane >> 1) * 8 : 0x7ffffff0u));
  fence();
  STAMP(16);
  fence();
  WAITV(0);
  asm volatile("" : "+v"(HA[4][0]), "+v"(HA[4][1]), "+v"(HA[5][0]), "+v"(HA[5][1]));
  acc[0][0] = mfma_bf(W4f[5][0], HA[4][0], acc[0][0]); acc[0][0] = mfma_bf(W4f[6][0], HA[5][0], acc[0][0]); PIN1(acc[0][0]);
  sb();
  acc[0][1] = mfma_bf(W4f[5][1], HA[4][0], acc[0][1]); acc[0][1] = mfma_bf(W4f[6][1], HA[5][0], acc[0][1]); PIN1(acc[0][1]);
  lds_st<f32x4>(rw0 + 0, acc[0][0]);
  sb();
  acc[0][2] = mfma_bf(W4f[5][2], HA[4][0], acc[0][2]); acc[0][2] = mfma_bf(W4f[6][2], HA[5][0], acc[0][2]); PIN1(acc[0][2]);
  lds_st<f32x4>(rw0 + 1024, acc[0][1]);
  sb();
  acc[0][3] = mfma_bf(W4f[5][3], HA[4][0], acc[0][3]); acc[0][3] = mfma_bf(W4f[6][3], HA[5][0], acc[0][3]); PIN1(acc[0][3]);
  lds_st<f32x4>(rw0 + 2048, acc[0][2]);
  sb();
  acc[1][0] = mfma_bf(W4f[5][0], HA[4][1], acc[1][0]); acc[1][0] = mfma_bf(W4f[6][0], HA[5][1], acc[1][0]); PIN1(acc[1][0]);
  lds_st<f32x4>(rw0 + 3072, acc[0][3]);
  sb();
  acc[1][1] = mfma_bf(W4f[5][1], HA[4][1], acc[1][1]); acc[1][1] = mfma_bf(W4f[6][1], HA[5][1], acc[1][1]); PIN1(acc[1][1]);
  lds_st<f32x4>(rw0 + 4096, acc[1][0]);
  sb();
  acc[1][2] = mfma_bf(W4f[5][2], HA[4][1], acc[1][2]); acc[1][2] = mfma_bf(W4f[6][2], HA[5][1], acc[1][2]); PIN1(acc[1][2]);
  lds_st<f32x4>(rw0 + 5120, acc[1][1]);
  sb();
  acc[1][3] = mfma_bf(W4f[5][3], HA[4][1], acc[1][3]); acc[1][3] = mfma_bf(W4f[6][3], HA[5][1], acc[1][3]); PIN1(acc[1][3]);
  lds_st<f32x4>(rw0 + 6144, acc[1][2]);
  sb();
  lds_st<f32x4>(rw0 + 7168, acc[1][3]);
  fence();
  POLL_CHECK(0, WS_FB + p * 128 + (lane >> 1) * 8, lane < 14);
  fence();
  bar();
  fence();
  STAMP(17);
  fence();
  HLD(HB[0][0], rT2, jj1);
  fence();
  HLD(HB[0][1], rT3, jj1);
  fence();
  HLD(HB[1][0], rT2, jj2);
  fence();
  HLD(HB[1][1], rT3, jj2);
  fence();
  HLD(HB[2][0], rT2, jj3);
  fence();
  so = WS_S + (64 * j + 8 * w) * 4096 + p * 128 - 16;
  const u32 vT = (lane >> 3) * 4096 + (((lane & 7) ^ (g << 1)) << 4);
  fence();
  DMA_ADD("sc1 ", so, rWS, vT, lw, LT_S, 16);
  fence();
  const u32 srow = 16 * c16 + 4 * g + q4;
  const u32 aS = LT_S + srow * 128 + l8, sS = (srow >> 1) & 3;
  GAS unsigned char* const po = (GAS unsigned char*)gout + (64 * p + tk) * (D * 2) + (64 * j + 16 * c16 + 4 * g) * 2;
  f32x4 pr[8];
  float hv[4], zz[4], dd[4];
  _Pragma("unroll") for (int v = 0; v < 8; ++v) pr[v] = lds_ld<f32x4>(rr0 + v * 8192);
  fence();
  acc[0][0] = mfma_bf(W4f[0][0], Hown[2], zero4); PIN1(acc[0][0]);
  acc[0][1] = mfma_bf(W4f[0][1], Hown[2], zero4); PIN1(acc[0][1]);
  sb();
  fence();
  HLD(HB[2][1], rT3, jj3);
  fence();
  acc[0][2] = mfma_bf(W4f[0][2], Hown[2], zero4); PIN1(acc[0][2]);
  acc[0][3] = mfma_bf(W4f[0][3], Hown[2], zero4); PIN1(acc[0][3]);
  sb();
  acc[1][0] = mfma_bf(W4f[0][0], Hown[3], zero4); PIN1(acc[1][0]);
  acc[1][1] = mfma_bf(W4f[0][1], Hown[3], zero4); PIN1(acc[1][1]);
  sb();
  fence();
  HLD(HB[3][0], rT2, jj4);
  fence();
  acc[1][2] = mfma_bf(W4f[0][2], Hown[3], zero4); PIN1(acc[1][2]);
  acc[1][3] = mfma_bf(W4f[0][3], Hown[3], zero4); PIN1(acc[1][3]);
  sb();
  _Pragma("unroll") for (int r = 0; r < 4; ++r) hv[r] = add1(add1(add1(pr[0][r], pr[1][r]), add1(pr[2][r], pr[3][r])), r & 1 ? bf_hi(r & 2 ? x1[0].y : x1[0].x) : bf_lo(r & 2 ? x1[0].y : x1[0].x));
  fence();
  WAITV(7);
  asm volatile("" : "+v"(HB[0][0]));
  acc[0][0] = mfma_bf(W4f[1][0], HB[0][0], acc[0][0]); PIN1(acc[0][0]);
  acc[0][1] = mfma_bf(W4f[1][1], HB[0][0], acc[0][1]); PIN1(acc[0][1]);
  sb();
  fence();
  HLD(HB[3][1], rT3, jj4);
  fence();
  acc[0][2] = mfma_bf(W4f[1][2], HB[0][0], acc[0][2]); PIN1(acc[0][2]);
  acc[0][3] = mfma_bf(W4f[1][3], HB[0][0], acc[0][3]); PIN1(acc[0][3]);
  sb();
  fence();
  WAITV(7);
  asm volatile("" : "+v"(HB[0][1]));
  acc[1][0] = mfma_bf(W4f[1][0], HB[0][1], acc[1][0]); PIN1(acc[1][0]);
  acc[1][1] = mfma_bf(W4f[1][1], HB[0][1], acc[1][1]); PIN1(acc[1][1]);
  sb();
  fence();
  HLD(HB[4][0], rT2, jj5);
  fence();
  acc[1][2] = mfma_bf(W4f[1][2], HB[0][1], acc[1][2]); PIN1(acc[1][2]);
  acc[1][3] = mfma_bf(W4f[1][3], HB[0][1], acc[1][3]); PIN1(acc[1][3]);
  sb();
  _Pragma("unroll") for (int r = 0; r < 4; ++r) zz[r] = add1(hv[r], add1(add1(pr[4][r], pr[5][r]), add1(pr[6][r], pr[7][r])));
  fence();
  WAITV(7);
  asm volatile("" : "+v"(HB[1][0]));
  acc[0][0] = mfma_bf(W4f[2][0], HB[1][0], acc[0][0]); PIN1(acc[0][0]);
  acc[0][1] = mfma_bf(W4f[2][1], HB[1][0], acc[0][1]); PIN1(acc[0][1]);
  sb();
  fence();
  HLD(HB[4][1], rT3, jj5);
  fence();
  acc[0][2] = mfma_bf(W4f[2][2], HB[1][0], acc[0][2]); PIN1(acc[0][2]);
  acc[0][3] = mfma_bf(W4f[2][3], HB[1][0], acc[0][3]); PIN1(acc[0][3]);
  sb();
  fence();
  WAITV(7);
  asm volatile("" : "+v"(HB[1][1]));
  acc[1][0] = mfma_bf(W4f[2][0], HB[1][1], acc[1][0]); PIN1(acc[1][0]);
  acc[1][1] = mfma_bf(W4f[2][1], HB[1][1], acc[1][1]); PIN1(acc[1][1]);
  sb();
  fence();
  HLD(HB[5][0], rT2, jj6);
  fence();
  acc[1][2] = mfma_bf(W4f[2][2], HB[1][1], acc[1][2]); PIN1(acc[1][2]);
  acc[1][3] = mfma_bf(W4f[2][3], HB[1][1], acc[1][3]); PIN1(acc[1][3]);
  sb();
  _Pragma("unroll") for (int r = 0; r < 4; ++r) dd[r] = add1t(__builtin_amdgcn_exp2f(mul1(zz[r], -1.4426950408889634f)), 1.f);
  fence();
  WAITV(7);
  asm volatile("" : "+v"(HB[2][0]));
  acc[0][0] = mfma_bf(W4f[3][0], HB[2][0], acc[0][0]); PIN1(acc[0][0]);
  acc[0][1] = mfma_bf(W4f[3][1], HB[2][0], acc[0][1]); PIN1(acc[0][1]);
  sb();
  fence();
  HLD(HB[5][1], rT3, jj6);
  fence();
  acc[0][2] = mfma_bf(W4f[3][2], HB[2][0], acc[0][2]); PIN1(acc[0][2]);
  acc[0][3] = mfma_bf(W4f[3][3], HB[2][0], acc[0][3]); PIN1(acc[0][3]);
  sb();
  _Pragma("unroll") for (int r = 0; r < 4; ++r) zz[r] = mul1t(zz[r], __builtin_amdgcn_rcpf(dd[r]));
  fence();
  WAITV(6);
  asm volatile("" : "+v"(HB[2][1]));
  acc[1][0] = mfma_bf(W4f[3][0], HB[2][1], acc[1][0]); PIN1(acc[1][0]);
  acc[1][1] = mfma_bf(W4f[3][1], HB[2][1], acc[1][1]); PIN1(acc[1][1]);
  acc[1][2] = mfma_bf(W4f[3][2], HB[2][1], acc[1][2]); PIN1(acc[1][2]);
  acc[1][3] = mfma_bf(W4f[3][3], HB[2][1], acc[1][3]); PIN1(acc[1][3]);
  sb();
  fence();
  asm volatile("s_barrier" ::: "memory");
  fence();
  const u32x2 sq0 = lds_tr(aS + ((ta ^ sS) << 5)), sq1 = lds_tr(aS + (((2 + ta) ^ sS) << 5));
  fence();
  WAITV(5);
  asm volatile("" : "+v"(HB[3][0]));
  acc[0][0] = mfma_bf(W4f[4][0], HB[3][0], acc[0][0]); PIN1(acc[0][0]);
  acc[0][1] = mfma_bf(W4f[4][1], HB[3][0], acc[0][1]); PIN1(acc[0][1]);
  acc[0][2] = mfma_bf(W4f[4][2], HB[3][0], acc[0][2]); PIN1(acc[0][2]);
  acc[0][3] = mfma_bf(W4f[4][3], HB[3][0], acc[0][3]); PIN1(acc[0][3]);
  sb();
  *(GAS u32x2*)po = (u32x2){pk_bf((f32x2){fma1(bf_lo(sq0.x), zz[0], bf_lo(xr[0].x)), fma1(bf_hi(sq0.x), zz[1], bf_hi(xr[0].x))}),
                            pk_bf((f32x2){fma1(bf_lo(sq0.y), zz[2], bf_lo(xr[0].y)), fma1(bf_hi(sq0.y), zz[3], bf_hi(xr[0].y))})};
  fence();
  WAITV(5);
  asm volatile("" : "+v"(HB[3][1]));
  acc[1][0] = mfma_bf(W4f[4][0], HB[3][1], acc[1][0]); PIN1(acc[1][0]);
  acc[1][1] = mfma_bf(W4f[4][1], HB[3][1], acc[1][1]); PIN1(acc[1][1]);
  acc[1][2] = mfma_bf(W4f[4][2], HB[3][1], acc[1][2]); PIN1(acc[1][2]);
  acc[1][3] = mfma_bf(W4f[4][3], HB[3][1], acc[1][3]); PIN1(acc[1][3]);
  sb();
  STAMP(18);
  fence();
  WAITV(1);
  asm volatile("" : "+v"(HB[4][0]), "+v"(HB[4][1]), "+v"(HB[5][0]), "+v"(HB[5][1]));
  acc[0][0] = mfma_bf(W4f[5][0], HB[4][0], acc[0][0]); acc[0][0] = mfma_bf(W4f[6][0], HB[5][0], acc[0][0]); PIN1(acc[0][0]);
  sb();
  acc[0][1] = mfma_bf(W4f[5][1], HB[4][0], acc[0][1]); acc[0][1] = mfma_bf(W4f[6][1], HB[5][0], acc[0][1]); PIN1(acc[0][1]);
  lds_st<f32x4>(rw1 + 0, acc[0][0]);
  sb();
  acc[0][2] = mfma_bf(W4f[5][2], HB[4][0], acc[0][2]); acc[0][2] = mfma_bf(W4f[6][2], HB[5][0], acc[0][2]); PIN1(acc[0][2]);
  lds_st<f32x4>(rw1 + 1024, acc[0][1]);
  sb();
  acc[0][3] = mfma_bf(W4f[5][3], HB[4][0], acc[0][3]); acc[0][3] = mfma_bf(W4f[6][3], HB[5][0], acc[0][3]); PIN1(acc[0][3]);
  lds_st<f32x4>(rw1 + 2048, acc[0][2]);
  sb();
  acc[1][0] = mfma_bf(W4f[5][0], HB[4][1], acc[1][0]); acc[1][0] = mfma_bf(W4f[6][0], HB[5][1], acc[1][0]); PIN1(acc[1][0]);
  lds_st<f32x4>(rw1 + 3072, acc[0][3]);
  sb();
  acc[1][1] = mfma_bf(W4f[5][1], HB[4][1], acc[1][1]); acc[1][1] = mfma_bf(W4f[6][1], HB[5][1], acc[1][1]); PIN1(acc[1][1]);
  lds_st<f32x4>(rw1 + 4096, acc[1][0]);
  sb();
  acc[1][2] = mfma_bf(W4f[5][2], HB[4][1], acc[1][2]); acc[1][2] = mfma_bf(W4f[6][2], HB[5][1], acc[1][2]); PIN1(acc[1][2]);
  lds_st<f32x4>(rw1 + 5120, acc[1][1]);
  sb();
  acc[1][3] = mfma_bf(W4f[5][3], HB[4][1], acc[1][3]); acc[1][3] = mfma_bf(W4f[6][3], HB[5][1], acc[1][3]); PIN1(acc[1][3]);
  lds_st<f32x4>(rw1 + 6144, acc[1][2]);
  sb();
  lds_st<f32x4>(rw1 + 7168, acc[1][3]);
  fence();
  bar();
  fence();
  STAMP(19);
  {
    const f32x4 h2 = ((lds_ld<f32x4>(rr1) + lds_ld<f32x4>(rr1 + 8192)) + (lds_ld<f32x4>(rr1 + 2 * 8192) + lds_ld<f32x4>(rr1 + 3 * 8192))) +
                     ((lds_ld<f32x4>(rr1 + 4 * 8192) + lds_ld<f32x4>(rr1 + 5 * 8192)) + (lds_ld<f32x4>(rr1 + 6 * 8192) + lds_ld<f32x4>(rr1 + 7 * 8192)));
    u32x2 ov;
#pragma unroll
    for (int r = 0; r < 2; ++r) {
      const f32x2 z = bf2(r ? x1[1].y : x1[1].x) + (r ? hi2(h2) : lo2(h2));
      const f32x2 t = z * bc(-1.4426950408889634f);
      const f32x2 d = (f32x2){__builtin_amdgcn_exp2f(t.x), __builtin_amdgcn_exp2f(t.y)} + bc(1.f);
      const f32x2 gt = z * (f32x2){__builtin_amdgcn_rcpf(d.x), __builtin_amdgcn_rcpf(d.y)};
      const u32 o = pk_bf(fma2(bf2(r ? sq1.y : sq1.x), gt, bf2(r ? xr[1].y : xr[1].x)));
      if (r) ov.y = o; else ov.x = o;
    }
    *(GAS u32x2*)(po + 32 * D * 2) = ov;
  }
  if (bid == 0) SIGNAL(WS_E);
  STAMP(20);
  asm volatile(".Ls4_end:");
}

extern "C" void kernel_launch(void* const* d_in, const int* in_sizes, int n_in, void* d_out, int out_size, void* d_ws, size_t ws_size,
                              hipStream_t stream) {
  (void)hipFuncSetAttribute((const void*)s4mix_kernel, hipFuncAttributeMaxDynamicSharedMemorySize, LDS_BYTES);
  s4mix_kernel<<<NWG, 512, LDS_BYTES, stream>>>((const u16*)d_in[0], (const u16*)d_in[10], (unsigned char*)d_ws, (const u16*)d_in[11], (const u16*)d_in[12],
                                                (const u16*)d_in[13], (const u16*)d_in[1], (const u16*)d_in[2], (const u16*)d_in[3], (const u16*)d_in[4],
                                                (const u16*)d_in[5], (const u16*)d_in[6], (const u16*)d_in[7], (const u16*)d_in[8], (u16*)d_out);
}
```
